```python
import torch

def _softmax(x, axis=-1):
    return torch.softmax(x, dim=axis)
def _gelu(x):
    return torch.nn.functional.gelu(x, approximate="tanh")
def _silu(x):
    return torch.nn.functional.silu(x)
def _sigmoid(x):
    return torch.sigmoid(x)
def _layernorm(x, axis=-1, eps=1e-5):
    m = x.mean(dim=axis, keepdim=True)
    v = x.var(dim=axis, keepdim=True, unbiased=False)
    return (x - m) / torch.sqrt(v + eps)
def _scatter_add(tgt, idx, src, axis=0):
    return tgt.clone().index_add_(axis, idx, src)
def _segment_sum(src, seg, n):
    out = torch.zeros((n,) + src.shape[1:], dtype=src.dtype)
    return out.index_add_(0, seg, src)
def _pad(x, pw):
    flat = tuple(v for pair in reversed(pw) for v in pair)
    return torch.nn.functional.pad(x, flat)

kernel_name = 'concat_heads__sliding_block__softmax_full_mha__prompt'

def setup_inputs():

    _w = (lambda a: (lambda t: t.to(torch.bfloat16) if t.is_floating_point() else t)(torch.as_tensor(a)))
    def _A_setup():
        B, S, D = 1, 2048, 640
        return {
            "x": _w((lambda *s: torch.randn(*s, dtype=torch.bfloat16) * 0.4)(B, S, D)),
            "wqkv": _w((lambda *s: torch.randn(*s, dtype=torch.bfloat16) * 0.4)(D, 3 * D)),
            "wo": _w((lambda *s: torch.randn(*s, dtype=torch.bfloat16) * 0.05)(D, D)),
        }
    _A_in = _A_setup()

    def _B_setup():
        B, S, D = 1, 2048, 640
        return {
            "x": _w((lambda *s: torch.randn(*s, dtype=torch.bfloat16) * 0.2)(B, S, D)),
            "wqkv": _w((lambda *s: torch.randn(*s, dtype=torch.bfloat16) * 0.4)(D, 3 * D)),
            "wo": _w((lambda *s: torch.randn(*s, dtype=torch.bfloat16) * 0.1)(D, D)),
        }
    _B_in = _B_setup()

    return {
        "A_x": _A_in["x"],
        "A_wqkv": _A_in["wqkv"],
        "A_wo": _A_in["wo"],
        "B_x": _B_in["x"],
        "B_wqkv": _B_in["wqkv"],
        "B_wo": _B_in["wo"],
    }

def reference(A_x, A_wqkv, A_wo, B_x, B_wqkv, B_wo):
    def _A_ref(x, wqkv, wo):
        B, S, D = x.shape
        H = 32; Dh = D // H; W = 256
        qkv = (lambda x, *d: x.permute(*d))((x @ wqkv).reshape(B, S, 3, H, Dh), 2, 0, 3, 1, 4)
        q, k, v = qkv[0], qkv[1], qkv[2]
        scores = torch.einsum("bhqd,bhkd->bhqk", q, k) * (Dh ** -0.5)
        idx = torch.arange(S)
        mask = (idx[:, None] - idx[None, :] >= 0) & (idx[:, None] - idx[None, :] < W)
        scores = torch.where(mask[None, None], scores, -1e9)
        p = _softmax(scores, axis=-1)
        o = torch.einsum("bhqk,bhkd->bhqd", p, v)
        return (lambda x, *d: x.permute(*d))(o, 0, 2, 1, 3).reshape(B, S, D) @ wo

    def _B_ref(x, wqkv, wo):
        B, S, D = x.shape
        H = 32; Dh = D // H
        qkv = (lambda x, *d: x.permute(*d))((x @ wqkv).reshape(B, S, 3, H, Dh), 2, 0, 3, 1, 4)
        q, k, v = qkv[0], qkv[1], qkv[2]
        scale = Dh ** -0.5
        scores = torch.einsum("bhqd,bhkd->bhqk", q, k) * scale
        p = _softmax(scores, axis=-1)
        o = torch.einsum("bhqk,bhkd->bhqd", p, v)
        return (lambda x, *d: x.permute(*d))(o, 0, 2, 1, 3).reshape(B, S, D) @ wo

    a = _A_ref(A_x, A_wqkv, A_wo)
    b = _B_ref(B_x, B_wqkv, B_wo)
    return torch.concatenate([a, b], axis=-1)
```

```cpp
#include <hip/hip_runtime.h>
#include <hip/hip_bf16.h>
#include <stdint.h>

typedef float f32x16 __attribute__((ext_vector_type(16)));
typedef float f32x4 __attribute__((ext_vector_type(4)));
typedef float f32x2 __attribute__((ext_vector_type(2)));
typedef short bf16x8 __attribute__((ext_vector_type(8)));
typedef short bf16x4 __attribute__((ext_vector_type(4)));
typedef unsigned u32x4 __attribute__((ext_vector_type(4)));
typedef unsigned u32x2 __attribute__((ext_vector_type(2)));
typedef __bf16 bf16x2_t __attribute__((ext_vector_type(2)));
typedef __attribute__((address_space(3))) const char* lds_cptr;
typedef __attribute__((address_space(3))) char* lds_ptr;

#define DI __device__ __forceinline__
#define SBAR() __builtin_amdgcn_sched_barrier(0)
#define PIN(x) asm volatile("" : "+v"(x))
#define MFMA32(a, b, c) __builtin_amdgcn_mfma_f32_32x32x16_bf16(a, b, c, 0, 0, 0)
#define MFMA32K8(a, b, c) __builtin_amdgcn_mfma_f32_32x32x8bf16_1k(a, b, c, 0, 0, 0)
#define MFMA16(a, b, c) __builtin_amdgcn_mfma_f32_16x16x32_bf16(a, b, c, 0, 0, 0)
#define EXP2(x) __builtin_amdgcn_exp2f(x)

constexpr int SEQ = 2048, DM = 640, NH = 32, N3 = 1920, NT = 64;
constexpr int QREC = 1280;
constexpr int KVREC = 2624;
constexpr int VOFF = 1280, VCH = 336;
constexpr size_t WS_Q = 0;
constexpr size_t WS_KV = (size_t)2 * NH * NT * QREC;
constexpr size_t WS_O = WS_KV + (size_t)2 * NH * NT * KVREC;
constexpr unsigned OOB = 0x40000000u;
constexpr float QSCALE = 1.4426950408889634f * 0.22360679774997896f;

DI unsigned cvtpk(float lo, float hi) { f32x2 v = {lo, hi}; bf16x2_t b = __builtin_convertvector(v, bf16x2_t); return __builtin_bit_cast(unsigned, b); }
DI bf16x8 ld128(lds_cptr p) { return *(const __attribute__((address_space(3))) bf16x8*)p; }
DI bf16x4 ld64(lds_cptr p) { return *(const __attribute__((address_space(3))) bf16x4*)p; }
DI bf16x4 ldtr(lds_cptr p) { return __builtin_bit_cast(bf16x4, __builtin_amdgcn_ds_read_tr16_b64_v4i16((__attribute__((address_space(3))) bf16x4*)p)); }
DI bf16x8 cat(bf16x4 a, bf16x4 b) { return __builtin_shufflevector(a, b, 0, 1, 2, 3, 4, 5, 6, 7); }
DI void dma16(unsigned m0v, unsigned voff, __amdgpu_buffer_rsrc_t srd, unsigned soff) {
  asm volatile("s_mov_b32 m0, %0\n\ts_nop 0\n\tbuffer_load_dwordx4 %1, %2, %3 offen lds" ::"s"(m0v), "v"(voff), "s"(srd), "s"(soff) : "memory");
}
DI bf16x8 gld128(const void* p) { bf16x8 r; asm volatile("global_load_dwordx4 %0, %1, off" : "=v"(r) : "v"(p) : "memory"); return r; }
DI bf16x4 gld64(const void* p) { bf16x4 r; asm volatile("global_load_dwordx2 %0, %1, off" : "=v"(r) : "v"(p) : "memory"); return r; }
DI __amdgpu_buffer_rsrc_t mksrd(const void* p, unsigned bytes) { return __builtin_amdgcn_make_buffer_rsrc((void*)p, 0, bytes, 0x00020000); }
#define WAITBAR(N) asm volatile("s_waitcnt vmcnt(" #N ") lgkmcnt(0)\n\ts_barrier" ::: "memory")

constexpr int K1_XB = 128 * 128;
constexpr int K1_WGRP = 8 * 480 + 128;
constexpr int K1_WB = 8 * K1_WGRP;
constexpr int K1_STAGE = K1_XB + K1_WB;
constexpr int K1_LDS = 3 * K1_STAGE;

template <int NCB, bool VT>
DI void k1_compute(f32x4 (&acc)[2][NCB], lds_cptr xb0, lds_cptr xb1, lds_cptr wb) {
#pragma unroll
  for (int ks = 0; ks < 2; ++ks) {
    const lds_cptr xb = ks ? xb1 : xb0;
    const bf16x8 xf0 = ld128(xb), xf1 = ld128(xb + 2048);
#pragma unroll
    for (int cb = 0; cb < NCB; ++cb) {
      const lds_cptr wp = wb + ks * 4 * K1_WGRP + cb * 32;
      const bf16x8 wf = cat(ldtr(wp), ldtr(wp + 4 * 480));
      if (VT) { acc[0][cb] = MFMA16(xf0, wf, acc[0][cb]); acc[1][cb] = MFMA16(xf1, wf, acc[1][cb]); }
      else { acc[0][cb] = MFMA16(wf, xf0, acc[0][cb]); acc[1][cb] = MFMA16(wf, xf1, acc[1][cb]); }
    }
  }
}

__global__ __launch_bounds__(512) void k1_qkv(const char* __restrict__ xA, const char* __restrict__ wA, const char* __restrict__ xB, const char* __restrict__ wB, char* __restrict__ ws) {
  extern __shared__ __attribute__((aligned(16))) char lds[];
  const int lane = threadIdx.x & 63, wid = __builtin_amdgcn_readfirstlane(threadIdx.x >> 6);
  const int g = blockIdx.x & 7, i = blockIdx.x >> 3;
  const int br = g & 1, mt = ((g >> 1) & 1) * 8 + (i & 7), j = (g >> 2) * 4 + (i >> 3);
  const __amdgpu_buffer_rsrc_t srdX = mksrd(br ? xB : xA, SEQ * DM * 2), srdW = mksrd(br ? wB : wA, DM * N3 * 2);
  const unsigned lds0 = (unsigned)(uintptr_t)lds;
  unsigned vx[2], vw[4];
#pragma unroll
  for (int e = 0; e < 2; ++e) { const int u = (2 * wid + e) * 64 + lane, tok = u >> 3, ch = (u & 7) ^ (tok & 7); vx[e] = (mt * 128 + tok) * 1280 + ch * 16; }
#pragma unroll
  for (int e = 0; e < 4; ++e) {
    const int n = min(wid * 4 + e, 30), u = n * 64 + lane, grp = u / 248, rem = u - grp * 248, r = rem / 30, ch = rem - r * 30;
    const unsigned col = ch < 20 ? 320 * j + ch * 16 : 2560 + 160 * j + (ch - 20) * 16;
    vw[e] = rem < 240 ? (grp * 8 + r) * 3840 + col : OOB;
  }
  auto issue = [&](int st, int slot) {
    const unsigned b = lds0 + slot * K1_STAGE;
    dma16(b + (2 * wid) * 1024, vx[0], srdX, st * 128);
    dma16(b + (2 * wid + 1) * 1024, vx[1], srdX, st * 128);
#pragma unroll
    for (int e = 0; e < 4; ++e) dma16(b + K1_XB + min(wid * 4 + e, 30) * 1024, vw[e], srdW, st * 64 * 3840);
  };
  issue(0, 0); issue(1, 1);
  const int wt = wid & 3, fr = lane & 15, gq = lane >> 4;
  const int tokl = wt * 32 + fr;
  const lds_cptr xb0 = (lds_cptr)lds + tokl * 128 + ((gq ^ (fr & 7)) << 4);
  const lds_cptr xb1 = (lds_cptr)lds + tokl * 128 + (((4 + gq) ^ (fr & 7)) << 4);
  const lds_cptr wb0 = (lds_cptr)lds + K1_XB + gq * K1_WGRP + (fr >> 2) * 480 + (fr & 3) * 8;
  const int tile = mt * 4 + wt;
  if (wid < 4) {
    f32x4 acc[2][10];
#pragma unroll
    for (int a = 0; a < 2; ++a)
#pragma unroll
      for (int c = 0; c < 10; ++c) acc[a][c] = f32x4{0.f, 0.f, 0.f, 0.f};
    int slot = 0;
    for (int st = 0; st < 10; ++st) {
      if (st < 9) WAITBAR(6); else WAITBAR(0);
      if (st < 8) issue(st + 2, slot == 0 ? 2 : slot - 1);
      const int so = slot * K1_STAGE;
      k1_compute<10, false>(acc, xb0 + so, xb1 + so, wb0 + so);
      slot = slot == 2 ? 0 : slot + 1;
    }
    const bool isq = j < 4;
    const float sc = isq ? QSCALE : 1.0f;
    char* base = ws + (isq ? WS_Q : WS_KV);
    const int rec = isq ? QREC : KVREC;
#pragma unroll
    for (int cb = 0; cb < 10; ++cb) {
      const int f = cb * 16 + 4 * gq, hs = f / 20, d0 = f - hs * 20, h = ((2 * j) * 4 + hs) & 31;
      char* rp = base + (size_t)((br * NH + h) * NT + tile) * rec;
#pragma unroll
      for (int tb = 0; tb < 2; ++tb) {
        const int tok = tb * 16 + fr;
        const int off = d0 < 16 ? (d0 >> 3) * 512 + tok * 16 + (d0 & 7) * 2 : 1024 + tok * 8;
        const u32x2 w = {cvtpk(acc[tb][cb][0] * sc, acc[tb][cb][1] * sc), cvtpk(acc[tb][cb][2] * sc, acc[tb][cb][3] * sc)};
        *(u32x2*)(rp + off) = w;
      }
    }
  } else {
    f32x4 acc[2][5];
#pragma unroll
    for (int a = 0; a < 2; ++a)
#pragma unroll
      for (int c = 0; c < 5; ++c) acc[a][c] = f32x4{0.f, 0.f, 0.f, 0.f};
    int slot = 0;
    for (int st = 0; st < 10; ++st) {
      if (st < 9) WAITBAR(6); else WAITBAR(0);
      if (st < 8) issue(st + 2, slot == 0 ? 2 : slot - 1);
      const int so = slot * K1_STAGE;
      k1_compute<5, true>(acc, xb0 + so, xb1 + so, wb0 + so + 320);
      slot = slot == 2 ? 0 : slot + 1;
    }
#pragma unroll
    for (int cb = 0; cb < 5; ++cb) {
      const int f = cb * 16 + fr, hs = f / 20, d = f - hs * 20, h = j * 4 + hs;
      char* rp = ws + WS_KV + (size_t)((br * NH + h) * NT + tile) * KVREC + VOFF + (gq & 1) * VCH + d * 16 + (gq >> 1) * 8;
#pragma unroll
      for (int tb = 0; tb < 2; ++tb) {
        const u32x2 w = {cvtpk(acc[tb][cb][0], acc[tb][cb][1]), cvtpk(acc[tb][cb][2], acc[tb][cb][3])};
        *(u32x2*)(rp + tb * 2 * VCH) = w;
        if (d == 19) *(u32x2*)(rp + tb * 2 * VCH + 16) = u32x2{0x3f803f80u, 0x3f803f80u};
      }
    }
  }
}

constexpr int TILEB = 3072;
constexpr int C4B = 512;
constexpr int A_MAIN = 0, A_C4 = 16 * TILEB, A_END = A_C4 + 16 * C4B;
constexpr int SLOT_C4 = 8 * TILEB, SLOTB = SLOT_C4 + 8 * C4B;
constexpr int K2_LDS = A_END + 3 * SLOTB;
constexpr float NEGINF = -__builtin_huge_valf();

DI int crow(int r, int hi) { return (r & 3) + 8 * (r >> 2) + 4 * hi; }
DI float rowmax16(const f32x16& s) {
  float m = fmaxf(fmaxf(s[0], s[1]), s[2]);
#pragma unroll
  for (int r = 3; r < 15; r += 2) m = fmaxf(fmaxf(m, s[r]), s[r + 1]);
  m = fmaxf(m, s[15]);
  auto rr = __builtin_amdgcn_permlane32_swap(__float_as_uint(m), __float_as_uint(m), false, false);
  return fmaxf(__uint_as_float(rr[0]), __uint_as_float(rr[1]));
}
DI bool acc_bad(const f32x16& o) {
  float t = 0.f;
#pragma unroll
  for (int r = 0; r < 12; ++r) t = __builtin_fmaf(o[r], 0.f, t);
  const bool bad = !(t == 0.f) || !(o[8] < 1.0e36f);
  return __any(bad);
}
template <bool WINDOW>
DI void slow_block(const char* __restrict__ kvh, int t_lo, int t_hi, int qrow, bf16x8 q16, bf16x4 q4, f32x16& o, float& negm) {
  const int lane = threadIdx.x & 63, r32 = lane & 31, hi = lane >> 5;
  float m = NEGINF;
  for (int pass = 0; pass < 2; ++pass) {
    for (int t = t_lo; t <= t_hi; ++t) {
      const char* rp = kvh + (size_t)t * KVREC;
      const bf16x8 k16 = *(const bf16x8*)(rp + hi * 512 + r32 * 16);
      const bf16x4 k4 = *(const bf16x4*)(rp + 1024 + r32 * 8);
      f32x16 s = {};
      s = MFMA32(k16, q16, s); s = MFMA32K8(k4, q4, s);
      if (WINDOW) {
#pragma unroll
        for (int r = 0; r < 16; ++r) { const int df = qrow - (t * 32 + crow(r, hi)); if (df < 0 || df >= 256) s[r] = NEGINF; }
      }
      if (pass == 0) {
#pragma unroll
        for (int r = 0; r < 16; ++r) m = fmaxf(m, s[r]);
      } else {
        const int vr = min(r32, 20);
        bf16x8 v0 = *(const bf16x8*)(rp + VOFF + hi * VCH + vr * 16), v1 = *(const bf16x8*)(rp + VOFF + (2 + hi) * VCH + vr * 16);
        u32x4 p0, p1;
#pragma unroll
        for (int r = 0; r < 4; ++r) { p0[r] = cvtpk(EXP2(s[2 * r] - m), EXP2(s[2 * r + 1] - m)); p1[r] = cvtpk(EXP2(s[8 + 2 * r] - m), EXP2(s[9 + 2 * r] - m)); }
        o = MFMA32(v0, __builtin_bit_cast(bf16x8, p0), o); o = MFMA32(v1, __builtin_bit_cast(bf16x8, p1), o);
      }
    }
    if (pass == 0) {
      auto rr = __builtin_amdgcn_permlane32_swap(__float_as_uint(m), __float_as_uint(m), false, false);
      m = fmaxf(__uint_as_float(rr[0]), __uint_as_float(rr[1]));
      o = f32x16{};
    }
  }
  negm = -m;
}
DI void store_o(const f32x16& o, unsigned short* op, int hi) {
  auto rr = __builtin_amdgcn_permlane32_swap(__float_as_uint(o[8]), __float_as_uint(o[8]), false, false);
  const float l = __uint_as_float(rr[1]);
  const float inv = 1.0f / l;
  const u32x2 w0 = {cvtpk(o[0] * inv, o[1] * inv), cvtpk(o[2] * inv, o[3] * inv)};
  const u32x2 w1 = {cvtpk(o[4] * inv, o[5] * inv), cvtpk(o[6] * inv, o[7] * inv)};
  *(u32x2*)(op + 4 * hi) = w0;
  *(u32x2*)(op + 8 + 4 * hi) = w1;
  if (!hi) { const u32x2 w2 = {cvtpk(o[8] * inv, o[9] * inv), cvtpk(o[10] * inv, o[11] * inv)}; *(u32x2*)(op + 16) = w2; }
}
#define EXP4(S_, i) do { S_[i] = EXP2(S_[i]); S_[i + 1] = EXP2(S_[i + 1]); S_[i + 2] = EXP2(S_[i + 2]); S_[i + 3] = EXP2(S_[i + 3]); PIN(S_); SBAR(); } while (0)
#define CVT4(P_, S_, i) do { P_[0] = cvtpk(S_[i], S_[i + 1]); P_[1] = cvtpk(S_[i + 2], S_[i + 3]); P_[2] = cvtpk(S_[i + 4], S_[i + 5]); P_[3] = cvtpk(S_[i + 6], S_[i + 7]); PIN(P_); SBAR(); } while (0)
#define BF8(P_) __builtin_bit_cast(bf16x8, P_)

__global__ __launch_bounds__(512) void k2_attn(char* __restrict__ ws) {
  extern __shared__ __attribute__((aligned(16))) char lds[];
  const int lane = threadIdx.x & 63, r32 = lane & 31, hi = lane >> 5;
  const int wid = __builtin_amdgcn_readfirstlane(threadIdx.x >> 6);
  const int g = blockIdx.x & 7, i = blockIdx.x >> 3;
  const int h = g * 4 + (i >> 3), T0 = (i & 7) * 8;
  const unsigned lds0 = (unsigned)(uintptr_t)lds;
  const __amdgpu_buffer_rsrc_t srd = mksrd(ws + WS_KV, 2u * NH * NT * KVREC);
  const unsigned v0 = lane * 16;
  const unsigned v1 = r32 < 21 ? VOFF + hi * VCH + r32 * 16 : OOB, v2 = r32 < 21 ? VOFF + (2 + hi) * VCH + r32 * 16 : OOB;
  const unsigned v3 = r32 < 16 ? hi * KVREC + 1024 + r32 * 16 : OOB;
  const unsigned recA = (unsigned)h * NT * KVREC, recB = (unsigned)(NH + h) * NT * KVREC;
  const char* qtA = ws + WS_Q + (size_t)h * NT * QREC;
  const char* qtB = ws + WS_Q + (size_t)(NH + h) * NT * QREC;
  bf16x8 q16a = gld128(qtA + (size_t)(T0 + wid) * QREC + hi * 512 + r32 * 16);
  bf16x4 q4a = gld64(qtA + (size_t)(T0 + wid) * QREC + 1024 + r32 * 8);
  bf16x8 q16[2]; bf16x4 q4[2];
#pragma unroll
  for (int b = 0; b < 2; ++b) {
    const char* qt = qtB + (size_t)(T0 + 2 * (wid & 3) + b) * QREC;
    q16[b] = gld128(qt + hi * 512 + r32 * 16);
    q4[b] = gld64(qt + 1024 + r32 * 8);
  }
  {
    const int t0 = max(T0 - 8 + 2 * wid, 0);
    const unsigned so = recA + t0 * KVREC, b = lds0 + A_MAIN + 2 * wid * TILEB;
    dma16(b, v0, srd, so); dma16(b + TILEB, v0, srd, so + KVREC);
    dma16(lds0 + A_C4 + 2 * wid * C4B, v3, srd, so);
    dma16(b + 1024, v1, srd, so); dma16(b + 2048, v2, srd, so);
    dma16(b + TILEB + 1024, v1, srd, so + KVREC); dma16(b + TILEB + 2048, v2, srd, so + KVREC);
  }
  auto issueB = [&](int st, int slot) {
    const unsigned so = recB + ((wid >> 2) * 32 + 4 * st + (wid & 3)) * KVREC, b = lds0 + A_END + slot * SLOTB;
    dma16(b + wid * TILEB, v0, srd, so); dma16(b + wid * TILEB + 1024, v1, srd, so); dma16(b + wid * TILEB + 2048, v2, srd, so);
    if (!(wid & 1)) dma16(b + SLOT_C4 + wid * C4B, v3, srd, so);
  };
  issueB(0, 0); issueB(1, 1);
  if (wid & 1) WAITBAR(6); else WAITBAR(8);
  asm volatile("" : "+v"(q16a), "+v"(q4a), "+v"(q16[0]), "+v"(q16[1]), "+v"(q4[0]), "+v"(q4[1]));
  if (hi) { q4a = bf16x4{0, 0, 0, 0}; q4[0] = bf16x4{0, 0, 0, 0}; q4[1] = bf16x4{0, 0, 0, 0}; }
  {
    const int Tq = T0 + wid;
    const int i0 = max(8 - Tq, 0);
    const lds_cptr kb = (lds_cptr)lds + A_MAIN + wid * TILEB + hi * 512 + r32 * 16;
    const lds_cptr k4b = (lds_cptr)lds + A_C4 + wid * C4B + hi * 256 + r32 * 8;
    const lds_cptr vb = (lds_cptr)lds + A_MAIN + wid * TILEB + 1024 + hi * 512 + r32 * 16;
    f32x16 cX, cF, cD;
    float negm;
    {
      const int ts = Tq ? 7 : 8;
      f32x16 s = {};
      s = MFMA32(ld128(kb + ts * TILEB), q16a, s); s = MFMA32K8(ld64(k4b + ts * C4B), q4a, s);
      if (Tq == 0) {
#pragma unroll
        for (int r = 0; r < 16; ++r) if (crow(r, hi) > r32) s[r] = NEGINF;
      }
      negm = -rowmax16(s);
#pragma unroll
      for (int r = 0; r < 16; ++r) {
        const bool dv = crow(r, hi) <= r32;
        cD[r] = dv ? negm : NEGINF;
        cF[r] = (dv || i0 > 0) ? NEGINF : negm;
        cX[r] = i0 > 1 ? NEGINF : negm;
      }
      PIN(cX); PIN(cF); PIN(cD);
    }
    f32x16 o = {}, sa, sb;
    bf16x8 k16, v0f, v1f, v1p; bf16x4 k4;
    u32x4 pw0, pw1 = {0, 0, 0, 0};
    sa = MFMA32(ld128(kb), q16a, cF); sa = MFMA32K8(ld64(k4b), q4a, sa);
    k16 = ld128(kb + TILEB); k4 = ld64(k4b + C4B); v0f = ld128(vb); v1p = ld128(vb + 1024);
#define ASTEP(i, SC, SN, CN) do { \
    if ((i) >= 1 && (i) <= 6) { if (i0 == (i) + 1) { _Pragma("unroll") for (int r = 0; r < 16; ++r) cX[r] = negm; PIN(cX); } } \
    SN = MFMA32(k16, q16a, CN); SBAR(); \
    EXP4(SC, 0); \
    o = MFMA32(v1p, BF8(pw1), o); SBAR(); \
    EXP4(SC, 4); \
    SN = MFMA32K8(k4, q4a, SN); SBAR(); \
    v1p = ld128(vb + (i) * TILEB + 1024); v1f = ld128(vb + ((i) + 1) * TILEB); \
    if ((i) < 7) { k4 = ld64(k4b + ((i) + 2) * C4B); k16 = ld128(kb + ((i) + 2) * TILEB); } SBAR(); \
    CVT4(pw0, SC, 0); EXP4(SC, 8); \
    o = MFMA32(v0f, BF8(pw0), o); SBAR(); \
    v0f = v1f; \
    EXP4(SC, 12); CVT4(pw1, SC, 8); \
    } while (0)
    ASTEP(0, sa, sb, cX); ASTEP(1, sb, sa, cX); ASTEP(2, sa, sb, cX); ASTEP(3, sb, sa, cX);
    ASTEP(4, sa, sb, cX); ASTEP(5, sb, sa, cX); ASTEP(6, sa, sb, cX); ASTEP(7, sb, sa, cD);
#undef ASTEP
    o = MFMA32(v1p, BF8(pw1), o);
    v1p = ld128(vb + 8 * TILEB + 1024);
    EXP4(sa, 0); EXP4(sa, 4); CVT4(pw0, sa, 0); EXP4(sa, 8); EXP4(sa, 12);
    o = MFMA32(v0f, BF8(pw0), o);
    CVT4(pw1, sa, 8);
    o = MFMA32(v1p, BF8(pw1), o);
    if (__builtin_expect(acc_bad(o), 0)) slow_block<true>(ws + WS_KV + recA, max(Tq - 8, 0), Tq, Tq * 32 + r32, q16a, q4a, o, negm);
    asm volatile("s_waitcnt vmcnt(0)" ::: "memory");
    store_o(o, (unsigned short*)(ws + WS_O) + (size_t)(Tq * 32 + r32) * DM + h * 20, hi);
  }
  {
    const int wr = wid & 3, ks = wid >> 2;
    const lds_cptr kb0 = (lds_cptr)lds + A_END + ks * 4 * TILEB + hi * 512 + r32 * 16;
    const lds_cptr k4b0 = (lds_cptr)lds + A_END + SLOT_C4 + ks * 4 * C4B + hi * 256 + r32 * 8;
    const lds_cptr vb0 = kb0 + 1024;
    asm volatile("s_barrier" ::: "memory");
    f32x16 negm[2], o[2], sa, sb;
    bf16x8 k16[2], v0f[2], v1f[2]; bf16x4 k4[2];
    u32x4 pw0[2], pw1[2];
    k16[0] = ld128(kb0); k4[0] = ld64(k4b0);
#pragma unroll
    for (int b = 0; b < 2; ++b) {
      f32x16 s = {};
      s = MFMA32(k16[0], q16[b], s); s = MFMA32K8(k4[0], q4[b], s);
      const float nm = -rowmax16(s);
#pragma unroll
      for (int r = 0; r < 16; ++r) negm[b][r] = nm;
      PIN(negm[b]);
      o[b] = f32x16{};
    }
    sa = MFMA32(k16[0], q16[0], negm[0]); sa = MFMA32K8(k4[0], q4[0], sa);
    v0f[0] = ld128(vb0); v1f[0] = ld128(vb0 + 1024);
    pw0[1] = u32x4{0, 0, 0, 0}; pw1[1] = u32x4{0, 0, 0, 0}; v0f[1] = v0f[0]; v1f[1] = v1f[0];
    int slot = 0;
    for (int st = 0; st < 8; ++st) {
      const int nslot = slot == 2 ? 0 : slot + 1;
      const int off = slot * SLOTB, noff = nslot * SLOTB;
      const lds_cptr kb = kb0 + off, k4b = k4b0 + off, vb = vb0 + off;
      const lds_cptr kbn = kb0 + noff, k4bn = k4b0 + noff, vbn = vb0 + noff;
#define BSTEP(i) do { \
      constexpr int cb = (i) & 1, nb = cb ^ 1; \
      if ((i) == 3) { asm volatile("s_waitcnt vmcnt(0)\n\ts_barrier" ::: "memory"); if (st < 6) issueB(st + 2, nslot == 2 ? 0 : nslot + 1); SBAR(); } \
      const lds_cptr pk = (i) < 3 ? kb + ((i) + 1) * TILEB : kbn, pk4 = (i) < 3 ? k4b + ((i) + 1) * C4B : k4bn, pv = (i) < 3 ? vb + ((i) + 1) * TILEB : vbn; \
      sb = MFMA32(k16[cb], q16[1], negm[1]); SBAR(); \
      EXP4(sa, 0); \
      o[1] = MFMA32(v0f[nb], BF8(pw0[1]), o[1]); SBAR(); \
      k4[nb] = ld64(pk4); k16[nb] = ld128(pk); SBAR(); \
      EXP4(sa, 4); \
      sb = MFMA32K8(k4[cb], q4[1], sb); SBAR(); \
      CVT4(pw0[0], sa, 0); EXP4(sa, 8); \
      o[1] = MFMA32(v1f[nb], BF8(pw1[1]), o[1]); SBAR(); \
      v1f[nb] = ld128(pv + 1024); v0f[nb] = ld128(pv); SBAR(); \
      EXP4(sa, 12); CVT4(pw1[0], sa, 8); \
      sa = MFMA32(k16[nb], q16[0], negm[0]); SBAR(); \
      EXP4(sb, 0); \
      o[0] = MFMA32(v0f[cb], BF8(pw0[0]), o[0]); SBAR(); \
      EXP4(sb, 4); \
      sa = MFMA32K8(k4[nb], q4[0], sa); SBAR(); \
      CVT4(pw0[1], sb, 0); EXP4(sb, 8); \
      o[0] = MFMA32(v1f[cb], BF8(pw1[0]), o[0]); SBAR(); \
      EXP4(sb, 12); CVT4(pw1[1], sb, 8); \
      } while (0)
      BSTEP(0); BSTEP(1); BSTEP(2); BSTEP(3);
#undef BSTEP
      slot = nslot;
    }
    o[1] = MFMA32(v0f[1], BF8(pw0[1]), o[1]);
    o[1] = MFMA32(v1f[1], BF8(pw1[1]), o[1]);
    float nm[2] = {negm[0][0], negm[1][0]};
#pragma unroll
    for (int b = 0; b < 2; ++b)
      if (__builtin_expect(acc_bad(o[b]), 0)) slow_block<false>(ws + WS_KV + recB, ks * 32, ks * 32 + 31, 0, q16[b], q4[b], o[b], nm[b]);
    float* mb = (float*)lds + wr * 2 * 13 * 64 + lane;
    if (ks == 1) {
#pragma unroll
      for (int b = 0; b < 2; ++b) {
#pragma unroll
        for (int r = 0; r < 12; ++r) mb[(b * 13 + r) * 64] = o[b][r];
        mb[(b * 13 + 12) * 64] = nm[b];
      }
    }
    __syncthreads();
    if (ks == 0) {
#pragma unroll
      for (int b = 0; b < 2; ++b) {
        const float n1 = mb[(b * 13 + 12) * 64], nx = fminf(nm[b], n1);
        const float f0 = EXP2(nx - nm[b]), f1 = EXP2(nx - n1);
#pragma unroll
        for (int r = 0; r < 12; ++r) o[b][r] = o[b][r] * f0 + mb[(b * 13 + r) * 64] * f1;
        store_o(o[b], (unsigned short*)(ws + WS_O) + (size_t)(SEQ + (T0 + 2 * wr + b) * 32 + r32) * DM + h * 20, hi);
      }
    }
  }
}

constexpr int K3_XB = 128 * 128;
constexpr int K3_WGRP = 8 * 160 + 128;
constexpr int K3_WB = 8 * K3_WGRP;
constexpr int K3_STAGE = K3_XB + K3_WB;
constexpr int K3_NSLOT = 5;
constexpr int K3_DUMMY = K3_NSLOT * K3_STAGE;
constexpr int K3_LDS = K3_DUMMY + 1024;

__global__ __launch_bounds__(512) void k3_out(const char* __restrict__ woA, const char* __restrict__ woB, const char* __restrict__ ws, unsigned short* __restrict__ out) {
  extern __shared__ __attribute__((aligned(16))) char lds[];
  const int lane = threadIdx.x & 63, wid = __builtin_amdgcn_readfirstlane(threadIdx.x >> 6);
  const int g = blockIdx.x & 7, i = blockIdx.x >> 3;
  const int br = g & 1, mt = (g >> 1) * 4 + (i & 3), j = i >> 2;
  const __amdgpu_buffer_rsrc_t srdX = mksrd(ws + WS_O + (size_t)br * SEQ * DM * 2, SEQ * DM * 2), srdW = mksrd(br ? woB : woA, DM * DM * 2);
  const unsigned lds0 = (unsigned)(uintptr_t)lds;
  unsigned vx[2], vw[2];
#pragma unroll
  for (int e = 0; e < 2; ++e) { const int u = (2 * wid + e) * 64 + lane, tok = u >> 3, ch = (u & 7) ^ (tok & 7); vx[e] = (mt * 128 + tok) * 1280 + ch * 16; }
#pragma unroll
  for (int e = 0; e < 2; ++e) {
    const int n = wid + 8 * e, u = n * 64 + lane, grp = u / 88, rem = u - grp * 88, r = rem / 10, ch = rem - r * 10;
    vw[e] = (n < 11 && rem < 80) ? (grp * 8 + r) * 1280 + j * 160 + ch * 16 : OOB;
  }
  auto issue = [&](int st, int slot) {
    const unsigned b = lds0 + slot * K3_STAGE;
    dma16(b + (2 * wid) * 1024, vx[0], srdX, st * 128);
    dma16(b + (2 * wid + 1) * 1024, vx[1], srdX, st * 128);
    dma16(b + K3_XB + wid * 1024, vw[0], srdW, st * 64 * 1280);
    dma16(wid < 3 ? b + K3_XB + (wid + 8) * 1024 : lds0 + K3_DUMMY, vw[1], srdW, st * 64 * 1280);
  };
  issue(0, 0); issue(1, 1); issue(2, 2); issue(3, 3);
  const int wt = wid & 3, ks = wid >> 2, fr = lane & 15, gq = lane >> 4;
  const lds_cptr xb0 = (lds_cptr)lds + (wt * 32 + fr) * 128 + (((ks * 4 + gq) ^ (fr & 7)) << 4);
  const lds_cptr wb0 = (lds_cptr)lds + K3_XB + (ks * 4 + gq) * K3_WGRP + (fr >> 2) * 160 + (fr & 3) * 8;
  f32x4 acc[2][5];
#pragma unroll
  for (int a = 0; a < 2; ++a)
#pragma unroll
    for (int c = 0; c < 5; ++c) acc[a][c] = f32x4{0.f, 0.f, 0.f, 0.f};
  int slot = 0;
  for (int st = 0; st < 10; ++st) {
    if (st < 7) WAITBAR(12); else if (st == 7) WAITBAR(8); else if (st == 8) WAITBAR(4); else WAITBAR(0);
    if (st < 6) issue(st + 4, slot == 0 ? K3_NSLOT - 1 : slot - 1);
    const int so = slot * K3_STAGE;
    const bf16x8 xf0 = ld128(xb0 + so), xf1 = ld128(xb0 + so + 2048);
#pragma unroll
    for (int cb = 0; cb < 5; ++cb) {
      const lds_cptr wp = wb0 + so + cb * 32;
      const bf16x8 wf = cat(ldtr(wp), ldtr(wp + 4 * 160));
      acc[0][cb] = MFMA16(wf, xf0, acc[0][cb]); acc[1][cb] = MFMA16(wf, xf1, acc[1][cb]);
    }
    slot = slot == K3_NSLOT - 1 ? 0 : slot + 1;
  }
  asm volatile("s_waitcnt lgkmcnt(0)\n\ts_barrier" ::: "memory");
  f32x4* rb = (f32x4*)lds + wt * 640 + lane;
  if (ks == 1) {
#pragma unroll
    for (int a = 0; a < 2; ++a)
#pragma unroll
      for (int c = 0; c < 5; ++c) rb[(a * 5 + c) * 64] = acc[a][c];
  }
  __syncthreads();
  if (ks == 0) {
#pragma unroll
    for (int a = 0; a < 2; ++a) {
      unsigned short* op = out + (size_t)(mt * 128 + wt * 32 + a * 16 + fr) * (2 * DM) + br * DM + j * 80 + 4 * gq;
#pragma unroll
      for (int c = 0; c < 5; ++c) {
        const f32x4 v = acc[a][c] + rb[(a * 5 + c) * 64];
        *(u32x2*)(op + c * 16) = u32x2{cvtpk(v[0], v[1]), cvtpk(v[2], v[3])};
      }
    }
  }
}

extern "C" void kernel_launch(void* const* d_in, const int* in_sizes, int n_in, void* d_out, int out_size, void* d_ws, size_t ws_size, hipStream_t stream) {
  (void)in_sizes; (void)n_in; (void)out_size; (void)ws_size;
  (void)hipFuncSetAttribute((const void*)k1_qkv, hipFuncAttributeMaxDynamicSharedMemorySize, K1_LDS);
  (void)hipFuncSetAttribute((const void*)k2_attn, hipFuncAttributeMaxDynamicSharedMemorySize, K2_LDS);
  (void)hipFuncSetAttribute((const void*)k3_out, hipFuncAttributeMaxDynamicSharedMemorySize, K3_LDS);
  char* ws = (char*)d_ws;
  k1_qkv<<<256, 512, K1_LDS, stream>>>((const char*)d_in[0], (const char*)d_in[1], (const char*)d_in[3], (const char*)d_in[4], ws);
  k2_attn<<<256, 512, K2_LDS, stream>>>(ws);
  k3_out<<<256, 512, K3_LDS, stream>>>((const char*)d_in[2], (const char*)d_in[5], ws, (unsigned short*)d_out);
}
```

```cpp
#include <hip/hip_runtime.h>
#include <hip/hip_bf16.h>
#include <stdint.h>

typedef float f32x16 __attribute__((ext_vector_type(16)));
typedef float f32x4 __attribute__((ext_vector_type(4)));
typedef float f32x2 __attribute__((ext_vector_type(2)));
typedef short bf16x8 __attribute__((ext_vector_type(8)));
typedef short bf16x4 __attribute__((ext_vector_type(4)));
typedef unsigned u32x4 __attribute__((ext_vector_type(4)));
typedef unsigned u32x2 __attribute__((ext_vector_type(2)));
typedef __bf16 bf16x2_t __attribute__((ext_vector_type(2)));
typedef __attribute__((address_space(3))) const char* lds_cptr;
typedef __attribute__((address_space(3))) char* lds_ptr;

#define DI __device__ __forceinline__
#define SBAR() __builtin_amdgcn_sched_barrier(0)
#define PIN(x) asm volatile("" : "+v"(x))
#define MFMA32(a, b, c) __builtin_amdgcn_mfma_f32_32x32x16_bf16(a, b, c, 0, 0, 0)
#define MFMA32K8(a, b, c) __builtin_amdgcn_mfma_f32_32x32x8bf16_1k(a, b, c, 0, 0, 0)
#define MFMA16(a, b, c) __builtin_amdgcn_mfma_f32_16x16x32_bf16(a, b, c, 0, 0, 0)
#define EXP2(x) __builtin_amdgcn_exp2f(x)

constexpr int SEQ = 2048, DM = 640, NH = 32, N3 = 1920, NT = 64;
constexpr int QREC = 1280;
constexpr int KVREC = 2624;
constexpr int VOFF = 1280, VCH = 336;
constexpr size_t WS_Q = 0;
constexpr size_t WS_KV = (size_t)2 * NH * NT * QREC;
constexpr size_t WS_O = WS_KV + (size_t)2 * NH * NT * KVREC;
constexpr unsigned OOB = 0x40000000u;
constexpr float QSCALE = 1.4426950408889634f * 0.22360679774997896f;

DI unsigned cvtpk(float lo, float hi) { f32x2 v = {lo, hi}; bf16x2_t b = __builtin_convertvector(v, bf16x2_t); return __builtin_bit_cast(unsigned, b); }
DI bf16x8 ld128(lds_cptr p) { return *(const __attribute__((address_space(3))) bf16x8*)p; }
DI bf16x4 ld64(lds_cptr p) { return *(const __attribute__((address_space(3))) bf16x4*)p; }
DI bf16x4 ldtr(lds_cptr p) { return __builtin_bit_cast(bf16x4, __builtin_amdgcn_ds_read_tr16_b64_v4i16((__attribute__((address_space(3))) bf16x4*)p)); }
DI bf16x8 cat(bf16x4 a, bf16x4 b) { return __builtin_shufflevector(a, b, 0, 1, 2, 3, 4, 5, 6, 7); }
DI void dma16(unsigned m0v, unsigned voff, __amdgpu_buffer_rsrc_t srd, unsigned soff) {
  asm volatile("s_mov_b32 m0, %0\n\ts_nop 0\n\tbuffer_load_dwordx4 %1, %2, %3 offen lds" ::"s"(m0v), "v"(voff), "s"(srd), "s"(soff) : "memory");
}
DI bf16x8 gld128(const void* p) { bf16x8 r; asm volatile("global_load_dwordx4 %0, %1, off" : "=v"(r) : "v"(p) : "memory"); return r; }
DI bf16x4 gld64(const void* p) { bf16x4 r; asm volatile("global_load_dwordx2 %0, %1, off" : "=v"(r) : "v"(p) : "memory"); return r; }
DI __amdgpu_buffer_rsrc_t mksrd(const void* p, unsigned bytes) { return __builtin_amdgcn_make_buffer_rsrc((void*)p, 0, bytes, 0x00020000); }
#define STAMP_INIT(K)
#define STAMP()
#define STAMP_END()
#define WAITBAR(N) asm volatile("s_waitcnt vmcnt(" #N ") lgkmcnt(0)\n\ts_barrier" ::: "memory")

constexpr int K1_XB = 128 * 128;
constexpr int K1_WGRP = 8 * 480 + 128;
constexpr int K1_WB = 8 * K1_WGRP;
constexpr int K1_STAGE = K1_XB + K1_WB;
constexpr int K1_LDS = 3 * K1_STAGE;

template <int NCB, bool VT>
DI void k1_compute(f32x4 (&acc)[2][NCB], lds_cptr xb0, lds_cptr xb1, lds_cptr wb) {
#pragma unroll
  for (int ks = 0; ks < 2; ++ks) {
    const lds_cptr xb = ks ? xb1 : xb0;
    const bf16x8 xf0 = ld128(xb), xf1 = ld128(xb + 2048);
#pragma unroll
    for (int cb = 0; cb < NCB; ++cb) {
      const lds_cptr wp = wb + ks * 4 * K1_WGRP + cb * 32;
      const bf16x8 wf = cat(ldtr(wp), ldtr(wp + 4 * 480));
      if (VT) { acc[0][cb] = MFMA16(xf0, wf, acc[0][cb]); acc[1][cb] = MFMA16(xf1, wf, acc[1][cb]); }
      else { acc[0][cb] = MFMA16(wf, xf0, acc[0][cb]); acc[1][cb] = MFMA16(wf, xf1, acc[1][cb]); }
    }
  }
}

__global__ __launch_bounds__(768) void k1_qkv(const char* __restrict__ xA, const char* __restrict__ wA, const char* __restrict__ xB, const char* __restrict__ wB, char* __restrict__ ws) {
  extern __shared__ __attribute__((aligned(16))) char lds[];
  const int lane = threadIdx.x & 63, wid = __builtin_amdgcn_readfirstlane(threadIdx.x >> 6);
  STAMP_INIT(0);
  const int g = blockIdx.x & 7, i = blockIdx.x >> 3;
  const int br = g & 1, mt = ((g >> 1) & 1) * 8 + (i & 7), j = (g >> 2) * 4 + (i >> 3);
  const unsigned lds0 = (unsigned)(uintptr_t)lds;
  if (wid >= 8) {
    const int lw = wid - 8;
    const __amdgpu_buffer_rsrc_t srdX = mksrd(br ? xB : xA, SEQ * DM * 2), srdW = mksrd(br ? wB : wA, DM * N3 * 2);
    unsigned vx[4], vw[8];
#pragma unroll
    for (int e = 0; e < 4; ++e) { const int u = (4 * lw + e) * 64 + lane, tok = u >> 3, ch = (u & 7) ^ (tok & 7); vx[e] = (mt * 128 + tok) * 1280 + ch * 16; }
#pragma unroll
    for (int e = 0; e < 8; ++e) {
      const int n = min(4 * e + lw, 30), u = n * 64 + lane, grp = u / 248, rem = u - grp * 248, r = rem / 30, ch = rem - r * 30;
      const unsigned col = ch < 20 ? 320 * j + ch * 16 : 2560 + 160 * j + (ch - 20) * 16;
      vw[e] = rem < 240 ? (grp * 8 + r) * 3840 + col : OOB;
    }
    auto issue = [&](int st, int slot) {
      const unsigned b = lds0 + slot * K1_STAGE;
#pragma unroll
      for (int e = 0; e < 4; ++e) {
        dma16(b + (4 * lw + e) * 1024, vx[e], srdX, st * 128);
        dma16(b + K1_XB + min(8 * e + lw, 30) * 1024, vw[2 * e], srdW, st * 64 * 3840);
        dma16(b + K1_XB + min(8 * e + 4 + lw, 30) * 1024, vw[2 * e + 1], srdW, st * 64 * 3840);
      }
    };
    issue(0, 0); issue(1, 1);
    STAMP();
    int slot = 0;
    for (int st = 0; st < 10; ++st) {
      if (st < 9) WAITBAR(12); else WAITBAR(0);
      if (st < 8) issue(st + 2, slot == 0 ? 2 : slot - 1);
      slot = slot == 2 ? 0 : slot + 1;
    }
    STAMP_END();
    return;
  }
  const int wt = wid & 3, fr = lane & 15, gq = lane >> 4;
  const int tokl = wt * 32 + fr;
  const lds_cptr xb0 = (lds_cptr)lds + tokl * 128 + ((gq ^ (fr & 7)) << 4);
  const lds_cptr xb1 = (lds_cptr)lds + tokl * 128 + (((4 + gq) ^ (fr & 7)) << 4);
  const lds_cptr wb0 = (lds_cptr)lds + K1_XB + gq * K1_WGRP + (fr >> 2) * 480 + (fr & 3) * 8;
  const int tile = mt * 4 + wt;
  if (wid < 4) {
    f32x4 acc[2][10];
#pragma unroll
    for (int a = 0; a < 2; ++a)
#pragma unroll
      for (int c = 0; c < 10; ++c) acc[a][c] = f32x4{0.f, 0.f, 0.f, 0.f};
    int slot = 0;
    for (int st = 0; st < 10; ++st) {
      asm volatile("s_waitcnt lgkmcnt(0)\n\ts_barrier" ::: "memory");
      STAMP();
      const int so = slot * K1_STAGE;
      k1_compute<10, false>(acc, xb0 + so, xb1 + so, wb0 + so);
      slot = slot == 2 ? 0 : slot + 1;
    }
    STAMP();
    const bool isq = j < 4;
    const float sc = isq ? QSCALE : 1.0f;
    const lds_ptr stg = (lds_ptr)lds + K1_STAGE + wt * 10240;
#pragma unroll
    for (int cb = 0; cb < 10; ++cb) {
      const int f = cb * 16 + 4 * gq, hs = f / 20, d0 = f - hs * 20;
#pragma unroll
      for (int tb = 0; tb < 2; ++tb) {
        const int tok = tb * 16 + fr;
        const int off = hs * 1280 + (d0 < 16 ? (d0 >> 3) * 512 + tok * 16 + (d0 & 7) * 2 : 1024 + tok * 8);
        *(__attribute__((address_space(3))) u32x2*)(stg + off) = u32x2{cvtpk(acc[tb][cb][0] * sc, acc[tb][cb][1] * sc), cvtpk(acc[tb][cb][2] * sc, acc[tb][cb][3] * sc)};
      }
    }
    char* base = ws + (isq ? WS_Q : WS_KV);
    const int rec = isq ? QREC : KVREC;
#pragma unroll
    for (int e = 0; e < 10; ++e) {
      const int u = e * 64 + lane, hs = u / 80, off = (u - hs * 80) * 16, h = (8 * j + hs) & 31;
      *(u32x4*)(base + (size_t)((br * NH + h) * NT + tile) * rec + off) = *(const __attribute__((address_space(3))) u32x4*)(stg + u * 16);
    }
  } else {
    f32x4 acc[2][5];
#pragma unroll
    for (int a = 0; a < 2; ++a)
#pragma unroll
      for (int c = 0; c < 5; ++c) acc[a][c] = f32x4{0.f, 0.f, 0.f, 0.f};
    int slot = 0;
    for (int st = 0; st < 10; ++st) {
      asm volatile("s_waitcnt lgkmcnt(0)\n\ts_barrier" ::: "memory");
      STAMP();
      const int so = slot * K1_STAGE;
      k1_compute<5, true>(acc, xb0 + so, xb1 + so, wb0 + so + 320);
      slot = slot == 2 ? 0 : slot + 1;
    }
    STAMP();
    const lds_ptr stg = (lds_ptr)lds + K1_STAGE + 40960 + wt * 5376;
    if (lane < 16) *(__attribute__((address_space(3))) u32x4*)(stg + (lane >> 2) * 1344 + (lane & 3) * VCH + 320) = u32x4{0x3f803f80u, 0x3f803f80u, 0x3f803f80u, 0x3f803f80u};
#pragma unroll
    for (int cb = 0; cb < 5; ++cb) {
      const int f = cb * 16 + fr, hs = f / 20, d = f - hs * 20;
#pragma unroll
      for (int tb = 0; tb < 2; ++tb)
        *(__attribute__((address_space(3))) u32x2*)(stg + hs * 1344 + (tb * 2 + (gq & 1)) * VCH + d * 16 + (gq >> 1) * 8) = u32x2{cvtpk(acc[tb][cb][0], acc[tb][cb][1]), cvtpk(acc[tb][cb][2], acc[tb][cb][3])};
    }
#pragma unroll
    for (int e = 0; e < 6; ++e) {
      const int u = e * 64 + lane, hs = u / 84, off = (u - hs * 84) * 16;
      if (u < 336) *(u32x4*)(ws + WS_KV + (size_t)((br * NH + j * 4 + hs) * NT + tile) * KVREC + VOFF + off) = *(const __attribute__((address_space(3))) u32x4*)(stg + u * 16);
    }
  }
  STAMP_END();
}

constexpr int TILEB = 3072;
constexpr int C4B = 512;
constexpr int A_MAIN = 0, A_C4 = 16 * TILEB, A_Q = A_C4 + 16 * C4B, A_END = A_Q + 8 * QREC;
constexpr int SLOT_C4 = 8 * TILEB, SLOTB = SLOT_C4 + 8 * C4B;
constexpr int K2_LDS = A_END + 3 * SLOTB;
constexpr float NEGINF = -__builtin_huge_valf();

DI int crow(int r, int hi) { return (r & 3) + 8 * (r >> 2) + 4 * hi; }
DI float rowmax16(const f32x16& s) {
  float m = fmaxf(fmaxf(s[0], s[1]), s[2]);
#pragma unroll
  for (int r = 3; r < 15; r += 2) m = fmaxf(fmaxf(m, s[r]), s[r + 1]);
  m = fmaxf(m, s[15]);
  auto rr = __builtin_amdgcn_permlane32_swap(__float_as_uint(m), __float_as_uint(m), false, false);
  return fmaxf(__uint_as_float(rr[0]), __uint_as_float(rr[1]));
}
DI bool acc_bad(const f32x16& o) {
  float t = 0.f;
#pragma unroll
  for (int r = 0; r < 12; ++r) t = __builtin_fmaf(o[r], 0.f, t);
  const bool bad = !(t == 0.f) || !(o[8] < 1.0e36f);
  return __any(bad);
}
template <bool WINDOW>
DI void slow_block(const char* __restrict__ kvh, int t_lo, int t_hi, int qrow, bf16x8 q16, bf16x4 q4, f32x16& o, float& negm) {
  const int lane = threadIdx.x & 63, r32 = lane & 31, hi = lane >> 5;
  float m = NEGINF;
  for (int pass = 0; pass < 2; ++pass) {
    for (int t = t_lo; t <= t_hi; ++t) {
      const char* rp = kvh + (size_t)t * KVREC;
      const bf16x8 k16 = *(const bf16x8*)(rp + hi * 512 + r32 * 16);
      const bf16x4 k4 = *(const bf16x4*)(rp + 1024 + r32 * 8);
      f32x16 s = {};
      s = MFMA32(k16, q16, s); s = MFMA32K8(k4, q4, s);
      if (WINDOW) {
#pragma unroll
        for (int r = 0; r < 16; ++r) { const int df = qrow - (t * 32 + crow(r, hi)); if (df < 0 || df >= 256) s[r] = NEGINF; }
      }
      if (pass == 0) {
#pragma unroll
        for (int r = 0; r < 16; ++r) m = fmaxf(m, s[r]);
      } else {
        const int vr = min(r32, 20);
        bf16x8 v0 = *(const bf16x8*)(rp + VOFF + hi * VCH + vr * 16), v1 = *(const bf16x8*)(rp + VOFF + (2 + hi) * VCH + vr * 16);
        u32x4 p0, p1;
#pragma unroll
        for (int r = 0; r < 4; ++r) { p0[r] = cvtpk(EXP2(s[2 * r] - m), EXP2(s[2 * r + 1] - m)); p1[r] = cvtpk(EXP2(s[8 + 2 * r] - m), EXP2(s[9 + 2 * r] - m)); }
        o = MFMA32(v0, __builtin_bit_cast(bf16x8, p0), o); o = MFMA32(v1, __builtin_bit_cast(bf16x8, p1), o);
      }
    }
    if (pass == 0) {
      auto rr = __builtin_amdgcn_permlane32_swap(__float_as_uint(m), __float_as_uint(m), false, false);
      m = fmaxf(__uint_as_float(rr[0]), __uint_as_float(rr[1]));
      o = f32x16{};
    }
  }
  negm = -m;
}
DI void store_o(const f32x16& o, unsigned short* op, int hi) {
  auto rr = __builtin_amdgcn_permlane32_swap(__float_as_uint(o[8]), __float_as_uint(o[8]), false, false);
  const float l = __uint_as_float(rr[1]);
  const float inv = 1.0f / l;
  const u32x2 w0 = {cvtpk(o[0] * inv, o[1] * inv), cvtpk(o[2] * inv, o[3] * inv)};
  const u32x2 w1 = {cvtpk(o[4] * inv, o[5] * inv), cvtpk(o[6] * inv, o[7] * inv)};
  *(u32x2*)(op + 4 * hi) = w0;
  *(u32x2*)(op + 8 + 4 * hi) = w1;
  if (!hi) { const u32x2 w2 = {cvtpk(o[8] * inv, o[9] * inv), cvtpk(o[10] * inv, o[11] * inv)}; *(u32x2*)(op + 16) = w2; }
}
#define EXP4(S_, i) do { S_[i] = EXP2(S_[i]); S_[i + 1] = EXP2(S_[i + 1]); S_[i + 2] = EXP2(S_[i + 2]); S_[i + 3] = EXP2(S_[i + 3]); PIN(S_); SBAR(); } while (0)
#define CVT4(P_, S_, i) do { P_[0] = cvtpk(S_[i], S_[i + 1]); P_[1] = cvtpk(S_[i + 2], S_[i + 3]); P_[2] = cvtpk(S_[i + 4], S_[i + 5]); P_[3] = cvtpk(S_[i + 6], S_[i + 7]); PIN(P_); SBAR(); } while (0)
#define BF8(P_) __builtin_bit_cast(bf16x8, P_)

__global__ __launch_bounds__(576) void k2_attn(char* __restrict__ ws) {
  extern __shared__ __attribute__((aligned(16))) char lds[];
  const int lane = threadIdx.x & 63, r32 = lane & 31, hi = lane >> 5;
  const int wid = __builtin_amdgcn_readfirstlane(threadIdx.x >> 6);
  const int g = blockIdx.x & 7, i = blockIdx.x >> 3;
  STAMP_INIT(1);
  const int h = g * 4 + (i >> 3), T0 = (i & 7) * 8;
  const unsigned recA = (unsigned)h * NT * KVREC, recB = (unsigned)(NH + h) * NT * KVREC;
  if (wid == 8) {
    const unsigned lds0 = (unsigned)(uintptr_t)lds;
    const __amdgpu_buffer_rsrc_t srd = mksrd(ws + WS_KV, 2u * NH * NT * KVREC), srdQ = mksrd(ws + WS_Q, 2u * NH * NT * QREC);
    const unsigned v0 = lane * 16;
    const unsigned v1 = r32 < 21 ? VOFF + hi * VCH + r32 * 16 : OOB, v2 = r32 < 21 ? VOFF + (2 + hi) * VCH + r32 * 16 : OOB;
    const unsigned v3 = r32 < 16 ? hi * KVREC + 1024 + r32 * 16 : OOB;
    auto tile2 = [&](unsigned b, unsigned c4, unsigned so) {
      dma16(b, v0, srd, so); dma16(b + TILEB, v0, srd, so + KVREC); dma16(c4, v3, srd, so);
      dma16(b + 1024, v1, srd, so); dma16(b + 2048, v2, srd, so);
      dma16(b + TILEB + 1024, v1, srd, so + KVREC); dma16(b + TILEB + 2048, v2, srd, so + KVREC);
    };
    auto issueB = [&](int st, int slot) {
      const unsigned b = lds0 + A_END + slot * SLOTB;
#pragma unroll
      for (int p = 0; p < 4; ++p) tile2(b + 2 * p * TILEB, b + SLOT_C4 + 2 * p * C4B, recB + ((p >> 1) * 32 + 4 * st + 2 * (p & 1)) * KVREC);
    };
    auto issueQ = [&](unsigned dst, unsigned so) {
#pragma unroll
      for (int e = 0; e < 10; ++e) dma16(dst + e * 1024, v0, srdQ, so + e * 1024);
    };
    issueQ(lds0 + A_MAIN, ((NH + h) * NT + T0) * QREC);
    issueB(0, 0); issueB(1, 1);
    STAMP();
    WAITBAR(28);
    int slot = 0;
    for (int st = 0; st < 8; ++st) {
      const int nslot = slot == 2 ? 0 : slot + 1;
      WAITBAR(0);
      if (st < 6) issueB(st + 2, nslot == 2 ? 0 : nslot + 1);
      tile2(lds0 + A_MAIN + 2 * st * TILEB, lds0 + A_C4 + 2 * st * C4B, recA + max(T0 - 8 + 2 * st, 0) * KVREC);
      if (st == 6) issueQ(lds0 + A_Q, (h * NT + T0) * QREC);
      slot = nslot;
    }
    WAITBAR(0);
    STAMP_END();
    return;
  }
  {
    const int wr = wid & 3, ks = wid >> 2;
    const lds_cptr kb0 = (lds_cptr)lds + A_END + ks * 4 * TILEB + hi * 512 + r32 * 16;
    const lds_cptr k4b0 = (lds_cptr)lds + A_END + SLOT_C4 + ks * 4 * C4B + hi * 256 + r32 * 8;
    const lds_cptr vb0 = kb0 + 1024;
    asm volatile("s_barrier" ::: "memory");
    STAMP();
    bf16x8 q16[2]; bf16x4 q4[2];
#pragma unroll
    for (int b = 0; b < 2; ++b) {
      const lds_cptr qp = (lds_cptr)lds + A_MAIN + (2 * wr + b) * QREC;
      q16[b] = ld128(qp + hi * 512 + r32 * 16);
      q4[b] = ld64(qp + 1024 + r32 * 8);
      if (hi) q4[b] = bf16x4{0, 0, 0, 0};
    }
    f32x16 negm[2], o[2], sa, sb;
    bf16x8 k16[2], v0f[2], v1f[2]; bf16x4 k4[2];
    u32x4 pw0[2], pw1[2];
    k16[0] = ld128(kb0); k4[0] = ld64(k4b0);
#pragma unroll
    for (int b = 0; b < 2; ++b) {
      f32x16 s = {};
      s = MFMA32(k16[0], q16[b], s); s = MFMA32K8(k4[0], q4[b], s);
      const float nm = -rowmax16(s);
#pragma unroll
      for (int r = 0; r < 16; ++r) negm[b][r] = nm;
      PIN(negm[b]);
      o[b] = f32x16{};
    }
    sa = MFMA32(k16[0], q16[0], negm[0]); sa = MFMA32K8(k4[0], q4[0], sa);
    v0f[0] = ld128(vb0); v1f[0] = ld128(vb0 + 1024);
    pw0[1] = u32x4{0, 0, 0, 0}; pw1[1] = u32x4{0, 0, 0, 0}; v0f[1] = v0f[0]; v1f[1] = v1f[0];
    int slot = 0;
    for (int st = 0; st < 8; ++st) {
      STAMP();
      const int nslot = slot == 2 ? 0 : slot + 1;
      const int off = slot * SLOTB, noff = nslot * SLOTB;
      const lds_cptr kb = kb0 + off, k4b = k4b0 + off, vb = vb0 + off;
      const lds_cptr kbn = kb0 + noff, k4bn = k4b0 + noff, vbn = vb0 + noff;
#define BSTEP(i) do { \
      constexpr int cb = (i) & 1, nb = cb ^ 1; \
      if ((i) == 3) { asm volatile("s_waitcnt lgkmcnt(0)\n\ts_barrier" ::: "memory"); SBAR(); } \
      const lds_cptr pk = (i) < 3 ? kb + ((i) + 1) * TILEB : kbn, pk4 = (i) < 3 ? k4b + ((i) + 1) * C4B : k4bn, pv = (i) < 3 ? vb + ((i) + 1) * TILEB : vbn; \
      __builtin_amdgcn_s_setprio(1); \
      sb = MFMA32(k16[cb], q16[1], negm[1]); SBAR(); \
      EXP4(sa, 0); \
      o[1] = MFMA32(v0f[nb], BF8(pw0[1]), o[1]); SBAR(); \
      k4[nb] = ld64(pk4); k16[nb] = ld128(pk); SBAR(); \
      EXP4(sa, 4); \
      sb = MFMA32K8(k4[cb], q4[1], sb); SBAR(); \
      CVT4(pw0[0], sa, 0); EXP4(sa, 8); \
      o[1] = MFMA32(v1f[nb], BF8(pw1[1]), o[1]); SBAR(); \
      v1f[nb] = ld128(pv + 1024); v0f[nb] = ld128(pv); SBAR(); \
      EXP4(sa, 12); CVT4(pw1[0], sa, 8); \
      __builtin_amdgcn_s_setprio(0); \
      sa = MFMA32(k16[nb], q16[0], negm[0]); SBAR(); \
      EXP4(sb, 0); \
      o[0] = MFMA32(v0f[cb], BF8(pw0[0]), o[0]); SBAR(); \
      EXP4(sb, 4); \
      sa = MFMA32K8(k4[nb], q4[0], sa); SBAR(); \
      CVT4(pw0[1], sb, 0); EXP4(sb, 8); \
      o[0] = MFMA32(v1f[cb], BF8(pw1[0]), o[0]); SBAR(); \
      EXP4(sb, 12); CVT4(pw1[1], sb, 8); \
      } while (0)
      BSTEP(0); BSTEP(1); BSTEP(2); BSTEP(3);
#undef BSTEP
      slot = nslot;
    }
    o[1] = MFMA32(v0f[1], BF8(pw0[1]), o[1]);
    o[1] = MFMA32(v1f[1], BF8(pw1[1]), o[1]);
    STAMP();
    float nm[2] = {negm[0][0], negm[1][0]};
#pragma unroll
    for (int b = 0; b < 2; ++b)
      if (__builtin_expect(acc_bad(o[b]), 0)) {
        const char* qt = ws + WS_Q + (size_t)((NH + h) * NT + T0 + 2 * wr + b) * QREC;
        (void)qt;
        slow_block<false>(ws + WS_KV + recB, ks * 32, ks * 32 + 31, 0, q16[b], q4[b], o[b], nm[b]);
      }
    float* mb = (float*)(lds + A_END) + wr * 2 * 13 * 64 + lane;
    if (ks == 1) {
#pragma unroll
      for (int b = 0; b < 2; ++b) {
#pragma unroll
        for (int r = 0; r < 12; ++r) mb[(b * 13 + r) * 64] = o[b][r];
        mb[(b * 13 + 12) * 64] = nm[b];
      }
    }
    asm volatile("s_waitcnt lgkmcnt(0)\n\ts_barrier" ::: "memory");
    if (ks == 0) {
#pragma unroll
      for (int b = 0; b < 2; ++b) {
        const float n1 = mb[(b * 13 + 12) * 64], nx = fminf(nm[b], n1);
        const float f0 = EXP2(nx - nm[b]), f1 = EXP2(nx - n1);
#pragma unroll
        for (int r = 0; r < 12; ++r) o[b][r] = o[b][r] * f0 + mb[(b * 13 + r) * 64] * f1;
        store_o(o[b], (unsigned short*)(ws + WS_O) + (size_t)(SEQ + (T0 + 2 * wr + b) * 32 + r32) * DM + h * 20, hi);
      }
    }
  }
  STAMP();
  {
    const int Tq = T0 + wid;
    const int i0 = max(8 - Tq, 0);
    const lds_cptr kb = (lds_cptr)lds + A_MAIN + wid * TILEB + hi * 512 + r32 * 16;
    const lds_cptr k4b = (lds_cptr)lds + A_C4 + wid * C4B + hi * 256 + r32 * 8;
    const lds_cptr vb = (lds_cptr)lds + A_MAIN + wid * TILEB + 1024 + hi * 512 + r32 * 16;
    const bf16x8 q16a = ld128((lds_cptr)lds + A_Q + wid * QREC + hi * 512 + r32 * 16);
    bf16x4 q4a = ld64((lds_cptr)lds + A_Q + wid * QREC + 1024 + r32 * 8);
    if (hi) q4a = bf16x4{0, 0, 0, 0};
    f32x16 cX, cF, cD;
    float negm;
    {
      const int ts = Tq ? 7 : 8;
      f32x16 s = {};
      s = MFMA32(ld128(kb + ts * TILEB), q16a, s); s = MFMA32K8(ld64(k4b + ts * C4B), q4a, s);
      if (Tq == 0) {
#pragma unroll
        for (int r = 0; r < 16; ++r) if (crow(r, hi) > r32) s[r] = NEGINF;
      }
      negm = -rowmax16(s);
#pragma unroll
      for (int r = 0; r < 16; ++r) {
        const bool dv = crow(r, hi) <= r32;
        cD[r] = dv ? negm : NEGINF;
        cF[r] = (dv || i0 > 0) ? NEGINF : negm;
        cX[r] = i0 > 1 ? NEGINF : negm;
      }
      PIN(cX); PIN(cF); PIN(cD);
    }
    f32x16 o = {}, sa, sb;
    bf16x8 k16, v0f, v1f, v1p; bf16x4 k4;
    u32x4 pw0, pw1 = {0, 0, 0, 0};
    sa = MFMA32(ld128(kb), q16a, cF); sa = MFMA32K8(ld64(k4b), q4a, sa);
    k16 = ld128(kb + TILEB); k4 = ld64(k4b + C4B); v0f = ld128(vb); v1p = ld128(vb + 1024);
#define ASTEP(i, SC, SN, CN) do { \
    if ((i) >= 1 && (i) <= 6) { if (i0 == (i) + 1) { _Pragma("unroll") for (int r = 0; r < 16; ++r) cX[r] = negm; PIN(cX); } } \
    __builtin_amdgcn_s_setprio(1); \
    SN = MFMA32(k16, q16a, CN); SBAR(); \
    EXP4(SC, 0); \
    o = MFMA32(v1p, BF8(pw1), o); SBAR(); \
    EXP4(SC, 4); \
    SN = MFMA32K8(k4, q4a, SN); SBAR(); \
    v1p = ld128(vb + (i) * TILEB + 1024); v1f = ld128(vb + ((i) + 1) * TILEB); \
    if ((i) < 7) { k4 = ld64(k4b + ((i) + 2) * C4B); k16 = ld128(kb + ((i) + 2) * TILEB); } SBAR(); \
    __builtin_amdgcn_s_setprio(0); \
    CVT4(pw0, SC, 0); EXP4(SC, 8); \
    o = MFMA32(v0f, BF8(pw0), o); SBAR(); \
    v0f = v1f; \
    EXP4(SC, 12); CVT4(pw1, SC, 8); \
    } while (0)
    ASTEP(0, sa, sb, cX); ASTEP(1, sb, sa, cX); ASTEP(2, sa, sb, cX); ASTEP(3, sb, sa, cX);
    ASTEP(4, sa, sb, cX); ASTEP(5, sb, sa, cX); ASTEP(6, sa, sb, cX); ASTEP(7, sb, sa, cD);
#undef ASTEP
    o = MFMA32(v1p, BF8(pw1), o);
    v1p = ld128(vb + 8 * TILEB + 1024);
    EXP4(sa, 0); EXP4(sa, 4); CVT4(pw0, sa, 0); EXP4(sa, 8); EXP4(sa, 12);
    o = MFMA32(v0f, BF8(pw0), o);
    CVT4(pw1, sa, 8);
    o = MFMA32(v1p, BF8(pw1), o);
    STAMP();
    if (__builtin_expect(acc_bad(o), 0)) slow_block<true>(ws + WS_KV + recA, max(Tq - 8, 0), Tq, Tq * 32 + r32, q16a, q4a, o, negm);
    store_o(o, (unsigned short*)(ws + WS_O) + (size_t)(Tq * 32 + r32) * DM + h * 20, hi);
  }
  STAMP_END();
}

constexpr int K3_XB = 128 * 128;
constexpr int K3_WGRP = 8 * 160 + 128;
constexpr int K3_WB = 8 * K3_WGRP;
constexpr int K3_STAGE = K3_XB + K3_WB;
constexpr int K3_NSLOT = 5;
constexpr int K3_LDS = K3_NSLOT * K3_STAGE;

__global__ __launch_bounds__(512) void k3_out(const char* __restrict__ woA, const char* __restrict__ woB, const char* __restrict__ ws, unsigned short* __restrict__ out) {
  extern __shared__ __attribute__((aligned(16))) char lds[];
  const int lane = threadIdx.x & 63, wid = __builtin_amdgcn_readfirstlane(threadIdx.x >> 6);
  const int g = blockIdx.x & 7, i = blockIdx.x >> 3;
  STAMP_INIT(2);
  const int br = g & 1, mt = (g >> 1) * 4 + (i & 3), j = i >> 2;
  const unsigned lds0 = (unsigned)(uintptr_t)lds;
  if (wid >= 4) {
    const int lw = wid - 4;
    const __amdgpu_buffer_rsrc_t srdX = mksrd(ws + WS_O + (size_t)br * SEQ * DM * 2, SEQ * DM * 2), srdW = mksrd(br ? woB : woA, DM * DM * 2);
    unsigned vx[4], vw[3];
#pragma unroll
    for (int e = 0; e < 4; ++e) { const int u = (4 * lw + e) * 64 + lane, tok = u >> 3, ch = (u & 7) ^ (tok & 7); vx[e] = (mt * 128 + tok) * 1280 + ch * 16; }
#pragma unroll
    for (int e = 0; e < 3; ++e) {
      const int n = min(4 * e + lw, 10), u = n * 64 + lane, grp = u / 88, rem = u - grp * 88, r = rem / 10, ch = rem - r * 10;
      vw[e] = rem < 80 ? (grp * 8 + r) * 1280 + j * 160 + ch * 16 : OOB;
    }
    auto issue = [&](int st, int slot) {
      const unsigned b = lds0 + slot * K3_STAGE;
      dma16(b + K3_XB + lw * 1024, vw[0], srdW, st * 64 * 1280);
      dma16(b + (4 * lw) * 1024, vx[0], srdX, st * 128);
      dma16(b + (4 * lw + 1) * 1024, vx[1], srdX, st * 128);
      dma16(b + K3_XB + (4 + lw) * 1024, vw[1], srdW, st * 64 * 1280);
      dma16(b + (4 * lw + 2) * 1024, vx[2], srdX, st * 128);
      dma16(b + (4 * lw + 3) * 1024, vx[3], srdX, st * 128);
      dma16(b + K3_XB + min(8 + lw, 10) * 1024, vw[2], srdW, st * 64 * 1280);
    };
    issue(0, 0); issue(1, 1); issue(2, 2); issue(3, 3);
    STAMP();
    int slot = 0;
    for (int st = 0; st < 10; ++st) {
      if (st < 7) WAITBAR(21); else if (st == 7) WAITBAR(14); else if (st == 8) WAITBAR(7); else WAITBAR(0);
      if (st < 6) issue(st + 4, slot == 0 ? K3_NSLOT - 1 : slot - 1);
      slot = slot == K3_NSLOT - 1 ? 0 : slot + 1;
    }
    STAMP_END();
    return;
  }
  const int wt = wid, fr = lane & 15, gq = lane >> 4;
  const lds_cptr xb0 = (lds_cptr)lds + (wt * 32 + fr) * 128 + ((gq ^ (fr & 7)) << 4);
  const lds_cptr xb1 = (lds_cptr)lds + (wt * 32 + fr) * 128 + (((4 + gq) ^ (fr & 7)) << 4);
  const lds_cptr wb0 = (lds_cptr)lds + K3_XB + gq * K3_WGRP + (fr >> 2) * 160 + (fr & 3) * 8;
  f32x4 acc[2][5];
#pragma unroll
  for (int a = 0; a < 2; ++a)
#pragma unroll
    for (int c = 0; c < 5; ++c) acc[a][c] = f32x4{0.f, 0.f, 0.f, 0.f};
  int slot = 0;
  for (int st = 0; st < 10; ++st) {
    asm volatile("s_waitcnt lgkmcnt(0)\n\ts_barrier" ::: "memory");
    STAMP();
    const int so = slot * K3_STAGE;
#pragma unroll
    for (int ks = 0; ks < 2; ++ks) {
      const lds_cptr xb = (ks ? xb1 : xb0) + so;
      const bf16x8 xf0 = ld128(xb), xf1 = ld128(xb + 2048);
#pragma unroll
      for (int cb = 0; cb < 5; ++cb) {
        const lds_cptr wp = wb0 + so + ks * 4 * K3_WGRP + cb * 32;
        const bf16x8 wf = cat(ldtr(wp), ldtr(wp + 4 * 160));
        acc[0][cb] = MFMA16(wf, xf0, acc[0][cb]); acc[1][cb] = MFMA16(wf, xf1, acc[1][cb]);
      }
    }
    slot = slot == K3_NSLOT - 1 ? 0 : slot + 1;
  }
  STAMP();
  const lds_ptr stg = (lds_ptr)lds + wt * 5120;
#pragma unroll
  for (int a = 0; a < 2; ++a)
#pragma unroll
    for (int c = 0; c < 5; ++c)
      *(__attribute__((address_space(3))) u32x2*)(stg + (a * 16 + fr) * 160 + c * 32 + gq * 8) = u32x2{cvtpk(acc[a][c][0], acc[a][c][1]), cvtpk(acc[a][c][2], acc[a][c][3])};
#pragma unroll
  for (int e = 0; e < 5; ++e) {
    const int u = e * 64 + lane, tok = u / 10, c = u - tok * 10;
    *(u32x4*)(out + (size_t)(mt * 128 + wt * 32 + tok) * (2 * DM) + br * DM + j * 80 + c * 8) = *(const __attribute__((address_space(3))) u32x4*)(stg + u * 16);
  }
  STAMP_END();
}

extern "C" void kernel_launch(void* const* d_in, const int* in_sizes, int n_in, void* d_out, int out_size, void* d_ws, size_t ws_size, hipStream_t stream) {
  (void)in_sizes; (void)n_in; (void)out_size; (void)ws_size;
  (void)hipFuncSetAttribute((const void*)k1_qkv, hipFuncAttributeMaxDynamicSharedMemorySize, K1_LDS);
  (void)hipFuncSetAttribute((const void*)k2_attn, hipFuncAttributeMaxDynamicSharedMemorySize, K2_LDS);
  (void)hipFuncSetAttribute((const void*)k3_out, hipFuncAttributeMaxDynamicSharedMemorySize, K3_LDS);
  char* ws = (char*)d_ws;
  k1_qkv<<<256, 768, K1_LDS, stream>>>((const char*)d_in[0], (const char*)d_in[1], (const char*)d_in[3], (const char*)d_in[4], ws);
  k2_attn<<<256, 576, K2_LDS, stream>>>(ws);
  k3_out<<<256, 512, K3_LDS, stream>>>((const char*)d_in[2], (const char*)d_in[5], ws, (unsigned short*)d_out);
}
```

```cpp
#include <hip/hip_runtime.h>
#include <hip/hip_bf16.h>
#include <stdint.h>

typedef float f32x16 __attribute__((ext_vector_type(16)));
typedef float f32x4 __attribute__((ext_vector_type(4)));
typedef float f32x2 __attribute__((ext_vector_type(2)));
typedef short bf16x8 __attribute__((ext_vector_type(8)));
typedef short bf16x4 __attribute__((ext_vector_type(4)));
typedef unsigned u32x4 __attribute__((ext_vector_type(4)));
typedef unsigned u32x2 __attribute__((ext_vector_type(2)));
typedef __bf16 bf16x2_t __attribute__((ext_vector_type(2)));
typedef __attribute__((address_space(3))) const char* lds_cptr;
typedef __attribute__((address_space(3))) char* lds_ptr;

#define DI __device__ __forceinline__
#define SBAR() __builtin_amdgcn_sched_barrier(0)
#define PIN(x) asm volatile("" : "+v"(x))
#define MFMA32(a, b, c) __builtin_amdgcn_mfma_f32_32x32x16_bf16(a, b, c, 0, 0, 0)
#define MFMA32K8(a, b, c) __builtin_amdgcn_mfma_f32_32x32x8bf16_1k(a, b, c, 0, 0, 0)
#define MFMA16(a, b, c) __builtin_amdgcn_mfma_f32_16x16x32_bf16(a, b, c, 0, 0, 0)
#define EXP2(x) __builtin_amdgcn_exp2f(x)

#define GEXP 0
#define K0 0
#define K0REP 1
#define REP1 1
#define REP2 1
#define REP3 1
#define ROT1 0
#define ROT3 1
#define PFD 4
#define PFT 0
#define PFC 1
#define PF1 0
#define PF3 0
constexpr int SEQ = 2048, DM = 640, NH = 32, N3 = 1920, NT = 64;
constexpr int QREC = 1280;
constexpr int KVREC = 2624;
constexpr int VOFF = 1280, VCH = 336;
constexpr size_t WS_Q = 0;
constexpr size_t WS_KV = (size_t)2 * NH * NT * QREC;
constexpr size_t WS_O = WS_KV + (size_t)2 * NH * NT * KVREC;
constexpr unsigned OOB = 0x40000000u;
constexpr float QSCALE = 1.4426950408889634f * 0.22360679774997896f;

DI unsigned cvtpk(float lo, float hi) { f32x2 v = {lo, hi}; bf16x2_t b = __builtin_convertvector(v, bf16x2_t); return __builtin_bit_cast(unsigned, b); }
DI bf16x8 ld128(lds_cptr p) { return *(const __attribute__((address_space(3))) bf16x8*)p; }
DI bf16x4 ld64(lds_cptr p) { return *(const __attribute__((address_space(3))) bf16x4*)p; }
DI bf16x4 ldtr(lds_cptr p) { return __builtin_bit_cast(bf16x4, __builtin_amdgcn_ds_read_tr16_b64_v4i16((__attribute__((address_space(3))) bf16x4*)p)); }
DI bf16x8 cat(bf16x4 a, bf16x4 b) { return __builtin_shufflevector(a, b, 0, 1, 2, 3, 4, 5, 6, 7); }
DI void dma16(unsigned m0v, unsigned voff, __amdgpu_buffer_rsrc_t srd, unsigned soff) {
  asm volatile("s_mov_b32 m0, %0\n\ts_nop 0\n\tbuffer_load_dwordx4 %1, %2, %3 offen lds" ::"s"(m0v), "v"(voff), "s"(srd), "s"(soff) : "memory");
}
DI bf16x8 gld128(const void* p) { bf16x8 r; asm volatile("global_load_dwordx4 %0, %1, off" : "=v"(r) : "v"(p) : "memory"); return r; }
DI bf16x4 gld64(const void* p) { bf16x4 r; asm volatile("global_load_dwordx2 %0, %1, off" : "=v"(r) : "v"(p) : "memory"); return r; }
DI void touch_line(unsigned m0v, const void* p) {
  asm volatile("s_mov_b32 m0, %0\n\ts_nop 0\n\tglobal_load_lds_dword %1, off" ::"s"(m0v), "v"(p) : "memory");
}
#define CODE_ADDR(SYM) ({ unsigned lo_, hi_; asm volatile("s_getpc_b64 s[40:41]\n\ts_add_u32 s40, s40, " SYM "@rel32@lo+4\n\ts_addc_u32 s41, s41, " SYM "@rel32@hi+12\n\ts_mov_b32 %0, s40\n\ts_mov_b32 %1, s41" \
  : "=s"(lo_), "=s"(hi_) :: "s40", "s41", "scc"); (const char*)(((unsigned long long)hi_ << 32) | lo_); })
constexpr int K1_CODE = 8192, K2_CODE = 13312, K3_CODE = 3584;
DI __amdgpu_buffer_rsrc_t mksrd(const void* p, unsigned bytes) { return __builtin_amdgcn_make_buffer_rsrc((void*)p, 0, bytes, 0x00020000); }
#define STAMP_EXTRA 0
#define STAMP_C()
#define STAMP_INIT(K, LDSOFF)
#define STAMP()
#define STAMP_END()
#define WAITBAR(N) asm volatile("s_waitcnt vmcnt(" #N ") lgkmcnt(0)\n\ts_barrier" ::: "memory")

constexpr int K1_XB = 128 * 128;
constexpr int K1_WB = 64 * 512;
constexpr int K1_STAGE = K1_XB + K1_WB;
constexpr int K1_LDS = 3 * K1_STAGE + 256;
DI int wswz(int k) { return ((k & 3) << 2) | (((k >> 3) & 1) << 1); }

#define LGKM(N) do { asm volatile("s_waitcnt lgkmcnt(" #N ")" ::: "memory"); SBAR(); } while (0)
template <int OFF> DI void rd_tr(bf16x4& r, unsigned a) { asm volatile("ds_read_b64_tr_b16 %0, %1 offset:%2" : "=v"(r) : "v"(a), "i"(OFF)); SBAR(); }
template <int OFF> DI void rd_128(bf16x8& r, unsigned a) { asm volatile("ds_read_b128 %0, %1 offset:%2" : "=v"(r) : "v"(a), "i"(OFF)); SBAR(); }
template <int G, bool VT, int KSO, int ROW4, class Hook>
DI void gemm_group16(f32x4 (&acc)[2][5], bf16x4 (&wl)[5], bf16x4 (&wh)[5], bf16x8 (&xf)[2][2], const unsigned (&w)[5], const unsigned (&wn)[5], unsigned xa, unsigned xb, int st, Hook&& hook) {
  constexpr bool LAST = G == 1;
  constexpr int WO = LAST ? 0 : KSO;
#define GM(c, a) do { if (VT) acc[a][c] = MFMA16(xf[G][a], cat(wl[c], wh[c]), acc[a][c]); else acc[a][c] = MFMA16(cat(wl[c], wh[c]), xf[G][a], acc[a][c]); SBAR(); } while (0)
#define RL(c) rd_tr<WO>(wl[c], LAST ? wn[c] : w[c])
#define RH(c) rd_tr<WO + ROW4>(wh[c], LAST ? wn[c] : w[c])
  LGKM(8);
  GM(0, 0);
  if (!LAST) rd_128<0>(xf[G ^ 1][0], xa);
  GM(0, 1);
  if (LAST) { asm volatile("s_waitcnt lgkmcnt(0)\n\ts_barrier" ::: "memory"); SBAR(); hook(st + 1); rd_128<0>(xf[G ^ 1][0], xa); }
  rd_128<0>(xf[G ^ 1][1], xb);
  RL(0);
  LGKM(9); GM(1, 0); RH(0); GM(1, 1); RL(1);
  LGKM(9); GM(2, 0); RH(1); GM(2, 1); RL(2);
  LGKM(9); GM(3, 0); RH(2); GM(3, 1); RL(3);
  LGKM(9); GM(4, 0); RH(3); GM(4, 1); RL(4); RH(4);
#undef GM
#undef RL
#undef RH
}
template <bool VT, int KSO, int ROW4, int STAGE, int NSLOT, class Hook>
DI void gemm_consume16(f32x4 (&acc)[2][5], const unsigned (&xb)[2][2], const unsigned (&wb)[5], Hook&& hook) {
  bf16x4 wl[5], wh[5]; bf16x8 xf[2][2];
  asm volatile("s_barrier" ::: "memory");
  hook(0);
  rd_128<0>(xf[0][0], xb[0][0]); rd_128<0>(xf[0][1], xb[0][1]);
#pragma unroll
  for (int c = 0; c < 5; ++c) { rd_tr<0>(wl[c], wb[c]); rd_tr<ROW4>(wh[c], wb[c]); }
  int so = 0;
#pragma nounroll
  for (int st = 0; st < 10; ++st) {
    const int son = so == (NSLOT - 1) * STAGE ? 0 : so + STAGE;
    unsigned w[5], wn[5];
#pragma unroll
    for (int c = 0; c < 5; ++c) { w[c] = wb[c] + so; wn[c] = wb[c] + son; }
    gemm_group16<0, VT, KSO, ROW4>(acc, wl, wh, xf, w, wn, xb[1][0] + so, xb[1][1] + so, st, hook);
    gemm_group16<1, VT, KSO, ROW4>(acc, wl, wh, xf, w, wn, xb[0][0] + son, xb[0][1] + son, st, hook);
    so = son;
  }
  LGKM(0);
}
template <int K, class Hook>
DI void gemm_step32(f32x16 (&acc)[5], bf16x4 (&wl)[5], bf16x4 (&wh)[5], bf16x8 (&xf)[2], const unsigned (&w)[5], const unsigned (&wn)[5], unsigned xnext, int st, Hook&& hook) {
  constexpr bool LAST = K == 3;
  constexpr int WO = LAST ? 0 : (K + 1) * 16 * 512;
#define GM(c) do { acc[c] = MFMA32(cat(wl[c], wh[c]), xf[K & 1], acc[c]); SBAR(); } while (0)
#define RW(c) do { rd_tr<WO>(wl[c], LAST ? wn[c] : w[c]); rd_tr<WO + 4 * 512>(wh[c], LAST ? wn[c] : w[c]); } while (0)
  LGKM(8);
  GM(0);
  if (LAST) { asm volatile("s_waitcnt lgkmcnt(0)\n\ts_barrier" ::: "memory"); SBAR(); hook(st + 1); }
  rd_128<0>(xf[(K & 1) ^ 1], xnext);
  RW(0);
  LGKM(9); GM(1); RW(1);
  LGKM(9); GM(2); RW(2);
  LGKM(9); GM(3); RW(3);
  LGKM(9); GM(4); RW(4);
#undef GM
#undef RW
}
template <int STAGE, int NSLOT, class Hook>
DI void gemm_consume32(f32x16 (&acc)[5], const unsigned (&xb)[4], const unsigned (&wb)[5], Hook&& hook) {
  bf16x4 wl[5], wh[5]; bf16x8 xf[2];
  asm volatile("s_barrier" ::: "memory");
  hook(0);
  rd_128<0>(xf[0], xb[0]);
#pragma unroll
  for (int c = 0; c < 5; ++c) { rd_tr<0>(wl[c], wb[c]); rd_tr<4 * 512>(wh[c], wb[c]); }
  int so = 0;
#pragma nounroll
  for (int st = 0; st < 10; ++st) {
    const int son = so == (NSLOT - 1) * STAGE ? 0 : so + STAGE;
    unsigned w[5], wn[5];
#pragma unroll
    for (int c = 0; c < 5; ++c) { w[c] = wb[c] + so; wn[c] = wb[c] + son; }
    gemm_step32<0>(acc, wl, wh, xf, w, wn, xb[1] + so, st, hook);
    gemm_step32<1>(acc, wl, wh, xf, w, wn, xb[2] + so, st, hook);
    gemm_step32<2>(acc, wl, wh, xf, w, wn, xb[3] + so, st, hook);
    gemm_step32<3>(acc, wl, wh, xf, w, wn, xb[0] + son, st, hook);
    so = son;
  }
  LGKM(0);
}

__global__ __launch_bounds__(768) void k1_qkv(const char* __restrict__ xA, const char* __restrict__ wA, const char* __restrict__ xB, const char* __restrict__ wB, char* __restrict__ ws,
                                              const char* __restrict__ woA, const char* __restrict__ woB, const char* __restrict__ outp) {
  extern __shared__ __attribute__((aligned(16))) char lds[];
  const int lane = threadIdx.x & 63, wid = __builtin_amdgcn_readfirstlane(threadIdx.x >> 6);
  STAMP_INIT(0, K1_LDS);
  const int g = blockIdx.x & 7, i = blockIdx.x >> 3;
  const int br = g & 1, mt = ((g >> 1) & 1) * 8 + (i & 7), j = (g >> 2) * 4 + (i >> 3);
  const unsigned lds0 = (unsigned)(uintptr_t)lds;
  if (wid >= 8) {
    const int lw = wid - 8;
    const __amdgpu_buffer_rsrc_t srdX = mksrd(br ? xB : xA, SEQ * DM * 2), srdW = mksrd(br ? wB : wA, DM * N3 * 2);
    unsigned vx[4], vw[8];
#pragma unroll
    for (int e = 0; e < 4; ++e) { const int u = (4 * lw + e) * 64 + lane, tok = u >> 3, ch = (u & 7) ^ ((tok >> 1) & 7); vx[e] = (mt * 128 + tok) * 1280 + ch * 16; }
#pragma unroll
    for (int e = 0; e < 8; ++e) {
      const int k = 2 * (4 * e + lw) + (lane >> 5), ch = (lane & 31) ^ wswz(k);
      const unsigned col = ch < 20 ? 320 * j + ch * 16 : 2560 + 160 * j + (ch - 20) * 16;
      vw[e] = ch < 30 ? k * 3840 + col : OOB;
    }
    const int rot = ROT1 ? (mt & 7) + ((j & 3) * 5 >> 1) : 0;
    auto issue = [&](int sq, int slot) {
      int st = sq + rot; st -= st >= 10 ? 10 : 0; st -= st >= 10 ? 10 : 0;
      const unsigned b = lds0 + slot * K1_STAGE;
#pragma unroll
      for (int e = 0; e < 4; ++e) {
        dma16(b + (4 * lw + e) * 1024, vx[e], srdX, st * 128);
        dma16(b + K1_XB + (8 * e + lw) * 1024, vw[2 * e], srdW, st * 64 * 3840);
        dma16(b + K1_XB + (8 * e + 4 + lw) * 1024, vw[2 * e + 1], srdW, st * 64 * 3840);
      }
    };
    STAMP();
    issue(0, 0); issue(1, 1);
    STAMP();
    int slot = 0;
#pragma nounroll
    for (int st = 0; st < 10; ++st) {
      if (st < 9) WAITBAR(12); else WAITBAR(0);
      STAMP();
      if (st < 8) issue(st + 2, slot == 0 ? 2 : slot - 1);
      slot = slot == 2 ? 0 : slot + 1;
    }
    asm volatile("s_barrier" ::: "memory");
    STAMP_END();
    return;
  }
  if (wid == 0 && i < 4) {
    const int ln = i * 64 + lane;
    const char* c1 = CODE_ADDR("_Z6k1_qkvPKcS0_S0_S0_PcS0_S0_S0_"); const char* c2 = CODE_ADDR("_Z7k2_attnPc"); const char* c3 = CODE_ADDR("_Z6k3_outPKcS0_S0_Pt");
    constexpr int n1 = K1_CODE / 128, n2 = K2_CODE / 128, n3 = K3_CODE / 128;
    const char* p = ln < n2 ? c2 + ln * 128 : ln < n2 + n3 ? c3 + (ln - n2) * 128 : c1 + min(ln - n2 - n3, n1 - 1) * 128;
    touch_line(lds0 + K1_LDS - 256, p);
  }
  const int wt = wid & 3;
  const int tile = mt * 4 + wt;
  const int fr = lane & 15, gq = lane >> 4;
  if (wid < 4) {
    const int t32 = lane & 31, hi = lane >> 5;
    unsigned xb[4], wb[5];
#pragma unroll
    for (int k = 0; k < 4; ++k) xb[k] = lds0 + (wt * 32 + t32) * 128 + (((2 * k + hi) ^ ((t32 >> 1) & 7)) << 4);
    {
      const int k = 8 * hi + (fr >> 2), sw = wswz(k);
#pragma unroll
      for (int c = 0; c < 5; ++c) wb[c] = lds0 + K1_XB + k * 512 + (((4 * c + 2 * (gq & 1) + ((fr & 3) >> 1)) ^ sw) << 4) + (fr & 1) * 8;
    }
    unsigned wo[5][4];
#pragma unroll
    for (int c = 0; c < 5; ++c)
#pragma unroll
      for (int q = 0; q < 4; ++q) {
        const int f = 32 * c + 8 * q + 4 * hi, hs = (f * 205) >> 12, d0 = f - hs * 20;
        wo[c][q] = hs * 1280 + (d0 < 16 ? (d0 >> 3) * 512 + t32 * 16 + (d0 & 7) * 2 : 1024 + t32 * 8);
      }
    const bool isq = j < 4;
    const int rec = isq ? QREC : KVREC;
    f32x16 acc[5];
#pragma unroll
    for (int c = 0; c < 5; ++c) acc[c] = f32x16{};
    gemm_consume32<K1_STAGE, 3>(acc, xb, wb, [&](int st) { if (st == 0) STAMP(); });
    STAMP();
    const lds_ptr stg = (lds_ptr)lds + wt * 10240;
    if (isq) {
#pragma unroll
      for (int c = 0; c < 5; ++c) acc[c] *= QSCALE;
    }
    const __amdgpu_buffer_rsrc_t srdO = mksrd(ws + (isq ? WS_Q : WS_KV), 2u * NH * NT * KVREC);
    const unsigned so = (unsigned)((br * NH + ((8 * j) & 31)) * NT + tile) * rec;
#pragma unroll
    for (int half = 0; half < 2; ++half) {
#pragma unroll
      for (int c = 0; c < 5; ++c)
#pragma unroll
        for (int q = 0; q < 4; ++q) {
          const int fq = 32 * c + 8 * q;
          if ((fq < 80) == (half == 0))
            *(__attribute__((address_space(3))) u32x2*)(stg + wo[c][q]) = u32x2{cvtpk(acc[c][4 * q], acc[c][4 * q + 1]), cvtpk(acc[c][4 * q + 2], acc[c][4 * q + 3])};
        }
#pragma unroll
      for (int e = 0; e < 5; ++e) {
        const int u = (half * 5 + e) * 64 + lane, hs = (u * 52429) >> 22, off = (u - hs * 80) * 16;
        const u32x4 v = *(const __attribute__((address_space(3))) u32x4*)(stg + u * 16);
        __builtin_amdgcn_raw_buffer_store_b128(v, srdO, hs * (NT * rec) + off, so, 0);
      }
    }
  } else {
    const int tok0 = wt * 32 + 16 * (fr >> 3) + (fr & 7);
    unsigned xb[2][2], wb[5];
#pragma unroll
    for (int ks = 0; ks < 2; ++ks)
#pragma unroll
      for (int a = 0; a < 2; ++a) { const int tok = tok0 + 8 * a; xb[ks][a] = lds0 + tok * 128 + (((4 * ks + gq) ^ ((tok >> 1) & 7)) << 4); }
    {
      const int k = 8 * gq + (fr >> 2), sw = wswz(k);
#pragma unroll
      for (int c = 0; c < 5; ++c) wb[c] = lds0 + K1_XB + k * 512 + (((20 + 2 * c + ((fr & 3) >> 1)) ^ sw) << 4) + (fr & 1) * 8;
    }
    unsigned vo[5];
#pragma unroll
    for (int c = 0; c < 5; ++c) { const int f = c * 16 + fr, hs = (f * 205) >> 12, d = f - hs * 20; vo[c] = hs * (NT * KVREC) + VOFF + gq * VCH + d * 16; }
    f32x4 acc[2][5];
#pragma unroll
    for (int a = 0; a < 2; ++a)
#pragma unroll
      for (int c = 0; c < 5; ++c) acc[a][c] = f32x4{0.f, 0.f, 0.f, 0.f};
    gemm_consume16<true, 32 * 512, 4 * 512, K1_STAGE, 3>(acc, xb, wb, [&](int st) { if (st == 0) STAMP(); });
    STAMP();
    const __amdgpu_buffer_rsrc_t srdO = mksrd(ws + WS_KV, 2u * NH * NT * KVREC);
    const unsigned so = (unsigned)((br * NH + 4 * j) * NT + tile) * KVREC;
#pragma unroll
    for (int c = 0; c < 5; ++c) {
      const u32x4 v = {cvtpk(acc[0][c][0], acc[0][c][1]), cvtpk(acc[0][c][2], acc[0][c][3]), cvtpk(acc[1][c][0], acc[1][c][1]), cvtpk(acc[1][c][2], acc[1][c][3])};
      __builtin_amdgcn_raw_buffer_store_b128(v, srdO, vo[c], so, 0);
    }
    if (lane < 16) __builtin_amdgcn_raw_buffer_store_b128(u32x4{0x3f803f80u, 0x3f803f80u, 0x3f803f80u, 0x3f803f80u}, srdO, (lane >> 2) * (NT * KVREC) + VOFF + (lane & 3) * VCH + 320, so, 0);
  }
  STAMP_END();
}

constexpr int TILEB = 3072;
constexpr int C4B = 512;
constexpr int A_MAIN = 0, A_C4 = 16 * TILEB, A_Q = A_C4 + 16 * C4B, A_END = A_Q + 8 * QREC;
constexpr int SLOT_C4 = 8 * TILEB, SLOTB = SLOT_C4 + 8 * C4B;
constexpr int K2_LDS = A_END + 3 * SLOTB;
constexpr float NEGINF = -__builtin_huge_valf();

DI int crow(int r, int hi) { return (r & 3) + 8 * (r >> 2) + 4 * hi; }
DI float rowmax16(const f32x16& s) {
  float m = fmaxf(fmaxf(s[0], s[1]), s[2]);
#pragma unroll
  for (int r = 3; r < 15; r += 2) m = fmaxf(fmaxf(m, s[r]), s[r + 1]);
  m = fmaxf(m, s[15]);
  auto rr = __builtin_amdgcn_permlane32_swap(__float_as_uint(m), __float_as_uint(m), false, false);
  return fmaxf(__uint_as_float(rr[0]), __uint_as_float(rr[1]));
}
DI bool acc_bad(const f32x16& o) {
  float t = 0.f;
#pragma unroll
  for (int r = 0; r < 12; ++r) t = __builtin_fmaf(o[r], 0.f, t);
  const bool bad = !(t == 0.f) || !(o[8] < 1.0e36f);
  return __any(bad);
}
template <bool WINDOW>
DI void slow_block(const char* __restrict__ kvh, int t_lo, int t_hi, int qrow, bf16x8 q16, bf16x4 q4, f32x16& o, float& negm) {
  const int lane = threadIdx.x & 63, r32 = lane & 31, hi = lane >> 5;
  float m = NEGINF;
  for (int pass = 0; pass < 2; ++pass) {
    for (int t = t_lo; t <= t_hi; ++t) {
      const char* rp = kvh + (size_t)t * KVREC;
      const bf16x8 k16 = *(const bf16x8*)(rp + hi * 512 + r32 * 16);
      const bf16x4 k4 = *(const bf16x4*)(rp + 1024 + r32 * 8);
      f32x16 s = {};
      s = MFMA32(k16, q16, s); s = MFMA32K8(k4, q4, s);
      if (WINDOW) {
#pragma unroll
        for (int r = 0; r < 16; ++r) { const int df = qrow - (t * 32 + crow(r, hi)); if (df < 0 || df >= 256) s[r] = NEGINF; }
      }
      if (pass == 0) {
#pragma unroll
        for (int r = 0; r < 16; ++r) m = fmaxf(m, s[r]);
      } else {
        const int vr = min(r32, 20);
        bf16x8 v0 = *(const bf16x8*)(rp + VOFF + hi * VCH + vr * 16), v1 = *(const bf16x8*)(rp + VOFF + (2 + hi) * VCH + vr * 16);
        u32x4 p0, p1;
#pragma unroll
        for (int r = 0; r < 4; ++r) { p0[r] = cvtpk(EXP2(s[2 * r] - m), EXP2(s[2 * r + 1] - m)); p1[r] = cvtpk(EXP2(s[8 + 2 * r] - m), EXP2(s[9 + 2 * r] - m)); }
        o = MFMA32(v0, __builtin_bit_cast(bf16x8, p0), o); o = MFMA32(v1, __builtin_bit_cast(bf16x8, p1), o);
      }
    }
    if (pass == 0) {
      auto rr = __builtin_amdgcn_permlane32_swap(__float_as_uint(m), __float_as_uint(m), false, false);
      m = fmaxf(__uint_as_float(rr[0]), __uint_as_float(rr[1]));
      o = f32x16{};
    }
  }
  negm = -m;
}
DI void store_o(const f32x16& o, char* op, lds_ptr stg) {
  const int lane = threadIdx.x & 63, r32 = lane & 31, hi = lane >> 5;
  auto rr = __builtin_amdgcn_permlane32_swap(__float_as_uint(o[8]), __float_as_uint(o[8]), false, false);
  const float inv = __builtin_amdgcn_rcpf(__uint_as_float(rr[1]));
  typedef __attribute__((address_space(3))) u32x2* lds_u2;
  *(lds_u2)(stg + r32 * 40 + 8 * hi) = u32x2{cvtpk(o[0] * inv, o[1] * inv), cvtpk(o[2] * inv, o[3] * inv)};
  *(lds_u2)(stg + r32 * 40 + 16 + 8 * hi) = u32x2{cvtpk(o[4] * inv, o[5] * inv), cvtpk(o[6] * inv, o[7] * inv)};
  if (!hi) *(lds_u2)(stg + r32 * 40 + 32) = u32x2{cvtpk(o[8] * inv, o[9] * inv), cvtpk(o[10] * inv, o[11] * inv)};
#pragma unroll
  for (int e = 0; e < 3; ++e) {
    const int p = e * 64 + lane, row = (p * 52429) >> 18;
    if (e < 2 || lane < 32) *(u32x2*)(op + row * 1280 + (p - row * 5) * 8) = *(lds_u2)(stg + p * 8);
  }
}
constexpr unsigned long long diag_mask(int r) {
  unsigned long long m = 0;
  for (int l = 0; l < 64; ++l) if ((r & 3) + 8 * (r >> 2) + 4 * (l >> 5) <= (l & 31)) m |= 1ull << l;
  return m;
}
#define EXP4(S_, i) do { S_[i] = EXP2(S_[i]); S_[i + 1] = EXP2(S_[i + 1]); S_[i + 2] = EXP2(S_[i + 2]); S_[i + 3] = EXP2(S_[i + 3]); PIN(S_); SBAR(); } while (0)
#define CVT4(P_, S_, i) do { P_[0] = cvtpk(S_[i], S_[i + 1]); P_[1] = cvtpk(S_[i + 2], S_[i + 3]); P_[2] = cvtpk(S_[i + 4], S_[i + 5]); P_[3] = cvtpk(S_[i + 6], S_[i + 7]); PIN(P_); SBAR(); } while (0)
#define BF8(P_) __builtin_bit_cast(bf16x8, P_)

__global__ __launch_bounds__(576) void k2_attn(char* __restrict__ ws) {
  extern __shared__ __attribute__((aligned(16))) char lds[];
  const int lane = threadIdx.x & 63, r32 = lane & 31, hi = lane >> 5;
  const int wid = __builtin_amdgcn_readfirstlane(threadIdx.x >> 6);
  const int g = blockIdx.x & 7, i = blockIdx.x >> 3;
  STAMP_INIT(1, K2_LDS);
  const int h = g * 4 + (i >> 3), T0 = (i & 7) * 8;
  const unsigned recA = (unsigned)h * NT * KVREC, recB = (unsigned)(NH + h) * NT * KVREC;
  if (wid == 8) {
    const unsigned lds0 = (unsigned)(uintptr_t)lds;
    const __amdgpu_buffer_rsrc_t srd = mksrd(ws + WS_KV, 2u * NH * NT * KVREC), srdQ = mksrd(ws + WS_Q, 2u * NH * NT * QREC);
    const unsigned v0 = lane * 16;
    const unsigned v1 = r32 < 21 ? VOFF + hi * VCH + r32 * 16 : OOB, v2 = r32 < 21 ? VOFF + (2 + hi) * VCH + r32 * 16 : OOB;
    const unsigned v3 = r32 < 16 ? hi * KVREC + 1024 + r32 * 16 : OOB;
    auto tile2 = [&](unsigned b, unsigned c4, unsigned so) {
      dma16(b, v0, srd, so); dma16(b + TILEB, v0, srd, so + KVREC); dma16(c4, v3, srd, so);
      dma16(b + 1024, v1, srd, so); dma16(b + 2048, v2, srd, so);
      dma16(b + TILEB + 1024, v1, srd, so + KVREC); dma16(b + TILEB + 2048, v2, srd, so + KVREC);
    };
    auto issueB = [&](int st, int slot) {
      const unsigned b = lds0 + A_END + slot * SLOTB;
#pragma unroll
      for (int p = 0; p < 4; ++p) tile2(b + 2 * p * TILEB, b + SLOT_C4 + 2 * p * C4B, recB + ((p >> 1) * 32 + 4 * st + 2 * (p & 1)) * KVREC);
    };
    auto issueQ = [&](unsigned dst, unsigned so) {
#pragma unroll
      for (int e = 0; e < 10; ++e) dma16(dst + e * 1024, v0, srdQ, so + e * 1024);
    };
    asm volatile("s_barrier" ::: "memory");
    STAMP();
    issueB(1, 1);
    int slot = 0;
#pragma nounroll
    for (int st = 0; st < 8; ++st) {
      const int nslot = slot == 2 ? 0 : slot + 1;
      WAITBAR(0);
      if (st < 6) issueB(st + 2, nslot == 2 ? 0 : nslot + 1);
      tile2(lds0 + A_MAIN + 2 * st * TILEB, lds0 + A_C4 + 2 * st * C4B, recA + max(T0 - 8 + 2 * st, 0) * KVREC);
      if (st == 6) issueQ(lds0 + A_Q, (h * NT + T0) * QREC);
      slot = nslot;
    }
    WAITBAR(0);
    STAMP_END();
    return;
  }
  {
    const int wr = wid & 3, ks = wid >> 2;
    {
      const unsigned lds0 = (unsigned)(uintptr_t)lds;
      const __amdgpu_buffer_rsrc_t srd = mksrd(ws + WS_KV, 2u * NH * NT * KVREC), srdQ = mksrd(ws + WS_Q, 2u * NH * NT * QREC);
      const unsigned so = recB + (ks * 32 + wr) * KVREC, b = lds0 + A_END + wid * TILEB;
      dma16(b, lane * 16, srd, so);
      if (!(wid & 1)) dma16(lds0 + A_END + SLOT_C4 + wid * C4B, r32 < 16 ? hi * KVREC + 1024 + r32 * 16 : OOB, srd, so);
      dma16(lds0 + A_MAIN + wid * 1024, lane * 16, srdQ, ((NH + h) * NT + T0) * QREC + wid * 1024);
      if (wid < 2) dma16(lds0 + A_MAIN + (8 + wid) * 1024, lane * 16, srdQ, ((NH + h) * NT + T0) * QREC + (8 + wid) * 1024);
      dma16(b + 1024, r32 < 21 ? VOFF + hi * VCH + r32 * 16 : OOB, srd, so);
      dma16(b + 2048, r32 < 21 ? VOFF + (2 + hi) * VCH + r32 * 16 : OOB, srd, so);
      asm volatile("s_waitcnt vmcnt(0)" ::: "memory");
    }
    const lds_cptr kb0 = (lds_cptr)lds + A_END + ks * 4 * TILEB + hi * 512 + r32 * 16;
    const lds_cptr k4b0 = (lds_cptr)lds + A_END + SLOT_C4 + ks * 4 * C4B + hi * 256 + r32 * 8;
    const lds_cptr vb0 = kb0 + 1024;
    asm volatile("s_barrier" ::: "memory");
    STAMP();
    bf16x8 q16[2]; bf16x4 q4[2];
#pragma unroll
    for (int b = 0; b < 2; ++b) {
      const lds_cptr qp = (lds_cptr)lds + A_MAIN + (2 * wr + b) * QREC;
      q16[b] = ld128(qp + hi * 512 + r32 * 16);
      q4[b] = ld64(qp + 1024 + r32 * 8);
      if (hi) q4[b] = bf16x4{0, 0, 0, 0};
    }
    f32x16 negm[2], o[2], sa, sb;
    bf16x8 k16[2], v0f[2], v1f[2]; bf16x4 k4[2];
    u32x4 pw0[2], pw1[2];
    k16[0] = ld128(kb0); k4[0] = ld64(k4b0);
#pragma unroll
    for (int b = 0; b < 2; ++b) {
      f32x16 s = {};
      s = MFMA32(k16[0], q16[b], s); s = MFMA32K8(k4[0], q4[b], s);
      const float nm = -rowmax16(s);
#pragma unroll
      for (int r = 0; r < 16; ++r) negm[b][r] = nm;
      PIN(negm[b]);
      o[b] = f32x16{};
    }
    sa = MFMA32(k16[0], q16[0], negm[0]); sa = MFMA32K8(k4[0], q4[0], sa);
    v0f[0] = ld128(vb0); v1f[0] = ld128(vb0 + 1024);
    pw0[1] = u32x4{0, 0, 0, 0}; pw1[1] = u32x4{0, 0, 0, 0}; v0f[1] = v0f[0]; v1f[1] = v1f[0];
    int slot = 0;
#pragma nounroll
    for (int st = 0; st < 8; ++st) {
      STAMP();
      const int nslot = slot == 2 ? 0 : slot + 1;
      const int off = slot * SLOTB, noff = nslot * SLOTB;
      const lds_cptr kb = kb0 + off, k4b = k4b0 + off, vb = vb0 + off;
      const lds_cptr kbn = kb0 + noff, k4bn = k4b0 + noff, vbn = vb0 + noff;
#define BSTEP(i) do { \
      constexpr int cb = (i) & 1, nb = cb ^ 1; \
      if ((i) == 3) { asm volatile("s_waitcnt lgkmcnt(0)\n\ts_barrier" ::: "memory"); SBAR(); } \
      const lds_cptr pk = (i) < 3 ? kb + ((i) + 1) * TILEB : kbn, pk4 = (i) < 3 ? k4b + ((i) + 1) * C4B : k4bn, pv = (i) < 3 ? vb + ((i) + 1) * TILEB : vbn; \
      __builtin_amdgcn_s_setprio(1); \
      sb = MFMA32(k16[cb], q16[1], negm[1]); SBAR(); \
      EXP4(sa, 0); \
      o[1] = MFMA32(v0f[nb], BF8(pw0[1]), o[1]); SBAR(); \
      k4[nb] = ld64(pk4); k16[nb] = ld128(pk); SBAR(); \
      EXP4(sa, 4); \
      sb = MFMA32K8(k4[cb], q4[1], sb); SBAR(); \
      CVT4(pw0[0], sa, 0); EXP4(sa, 8); \
      o[1] = MFMA32(v1f[nb], BF8(pw1[1]), o[1]); SBAR(); \
      v1f[nb] = ld128(pv + 1024); v0f[nb] = ld128(pv); SBAR(); \
      EXP4(sa, 12); CVT4(pw1[0], sa, 8); \
      __builtin_amdgcn_s_setprio(0); \
      sa = MFMA32(k16[nb], q16[0], negm[0]); SBAR(); \
      EXP4(sb, 0); \
      o[0] = MFMA32(v0f[cb], BF8(pw0[0]), o[0]); SBAR(); \
      EXP4(sb, 4); \
      sa = MFMA32K8(k4[nb], q4[0], sa); SBAR(); \
      CVT4(pw0[1], sb, 0); EXP4(sb, 8); \
      o[0] = MFMA32(v1f[cb], BF8(pw1[0]), o[0]); SBAR(); \
      EXP4(sb, 12); CVT4(pw1[1], sb, 8); \
      } while (0)
      BSTEP(0); BSTEP(1); BSTEP(2); BSTEP(3);
#undef BSTEP
      slot = nslot;
    }
    o[1] = MFMA32(v0f[1], BF8(pw0[1]), o[1]);
    o[1] = MFMA32(v1f[1], BF8(pw1[1]), o[1]);
    STAMP();
    float nm[2] = {negm[0][0], negm[1][0]};
#pragma unroll
    for (int b = 0; b < 2; ++b)
      if (__builtin_expect(acc_bad(o[b]), 0)) {
        slow_block<false>(ws + WS_KV + recB, ks * 32, ks * 32 + 31, 0, q16[b], q4[b], o[b], nm[b]);
      }
    typedef __attribute__((address_space(3))) f32x4* lds_f4;
    typedef __attribute__((address_space(3))) float* lds_f;
    const lds_ptr mb = (lds_ptr)lds + A_END + wr * 2 * 3328 + lane * 16, mn = (lds_ptr)lds + A_END + wr * 2 * 3328 + 3072 + lane * 4;
    if (ks == 1) {
#pragma unroll
      for (int b = 0; b < 2; ++b) {
        *(lds_f4)(mb + b * 3328) = f32x4{o[b][0], o[b][1], o[b][2], o[b][3]};
        *(lds_f4)(mb + b * 3328 + 1024) = f32x4{o[b][4], o[b][5], o[b][6], o[b][7]};
        *(lds_f4)(mb + b * 3328 + 2048) = f32x4{o[b][8], o[b][9], o[b][10], o[b][11]};
        *(lds_f)(mn + b * 3328) = nm[b];
      }
    }
    asm volatile("s_waitcnt lgkmcnt(0)\n\ts_barrier" ::: "memory");
    if (ks == 0) {
#pragma unroll
      for (int b = 0; b < 2; ++b) {
        const f32x4 p0 = *(lds_f4)(mb + b * 3328), p1 = *(lds_f4)(mb + b * 3328 + 1024), p2 = *(lds_f4)(mb + b * 3328 + 2048);
        const float n1 = *(lds_f)(mn + b * 3328), nx = fminf(nm[b], n1);
        const float f0 = EXP2(nx - nm[b]), f1 = EXP2(nx - n1);
#pragma unroll
        for (int r = 0; r < 4; ++r) { o[b][r] = o[b][r] * f0 + p0[r] * f1; o[b][4 + r] = o[b][4 + r] * f0 + p1[r] * f1; o[b][8 + r] = o[b][8 + r] * f0 + p2[r] * f1; }
        store_o(o[b], ws + WS_O + (size_t)(SEQ + (T0 + 2 * wr + b) * 32) * (DM * 2) + h * 40, (lds_ptr)lds + A_END + 2 * SLOTB + wid * 1280);
      }
    }
  }
  STAMP();
  {
    const int Tq = T0 + wid;
    const int i0 = max(8 - Tq, 0);
    const lds_cptr kb = (lds_cptr)lds + A_MAIN + wid * TILEB + hi * 512 + r32 * 16;
    const lds_cptr k4b = (lds_cptr)lds + A_C4 + wid * C4B + hi * 256 + r32 * 8;
    const lds_cptr vb = (lds_cptr)lds + A_MAIN + wid * TILEB + 1024 + hi * 512 + r32 * 16;
    const bf16x8 q16a = ld128((lds_cptr)lds + A_Q + wid * QREC + hi * 512 + r32 * 16);
    bf16x4 q4a = ld64((lds_cptr)lds + A_Q + wid * QREC + 1024 + r32 * 8);
    if (hi) q4a = bf16x4{0, 0, 0, 0};
    f32x16 cX, cF, cD;
    float negm;
    {
      const int ts = Tq ? 7 : 8;
      f32x16 s = {};
      s = MFMA32(ld128(kb + ts * TILEB), q16a, s); s = MFMA32K8(ld64(k4b + ts * C4B), q4a, s);
      if (__builtin_expect(Tq == 0, 0)) {
#pragma unroll
        for (int r = 0; r < 16; ++r) if (crow(r, hi) > r32) s[r] = NEGINF;
      }
      negm = -rowmax16(s);
      const float nF = i0 > 0 ? NEGINF : negm, nX = i0 > 1 ? NEGINF : negm;
#define MK(r) do { const bool dv = __builtin_amdgcn_inverse_ballot_w64(diag_mask(r)); cD[r] = dv ? negm : NEGINF; cF[r] = dv ? NEGINF : nF; cX[r] = nX; } while (0)
      MK(0); MK(1); MK(2); MK(3); MK(4); MK(5); MK(6); MK(7); MK(8); MK(9); MK(10); MK(11); MK(12); MK(13); MK(14); MK(15);
#undef MK
      PIN(cX); PIN(cF); PIN(cD);
    }
    f32x16 o = {}, sa, sb;
    bf16x8 k16, v0f, v1f, v1p; bf16x4 k4;
    u32x4 pw0, pw1 = {0, 0, 0, 0};
    sa = MFMA32(ld128(kb), q16a, cF); sa = MFMA32K8(ld64(k4b), q4a, sa);
    k16 = ld128(kb + TILEB); k4 = ld64(k4b + C4B); v0f = ld128(vb); v1p = ld128(vb + 1024);
#define ASTEP(i, SC, SN, CN) do { \
    if ((i) >= 1 && (i) <= 6) { if (__builtin_expect(i0 == (i) + 1, 0)) { _Pragma("unroll") for (int r = 0; r < 16; ++r) cX[r] = negm; PIN(cX); } } \
    __builtin_amdgcn_s_setprio(1); \
    SN = MFMA32(k16, q16a, CN); SBAR(); \
    EXP4(SC, 0); \
    o = MFMA32(v1p, BF8(pw1), o); SBAR(); \
    EXP4(SC, 4); \
    SN = MFMA32K8(k4, q4a, SN); SBAR(); \
    v1p = ld128(vb + (i) * TILEB + 1024); v1f = ld128(vb + ((i) + 1) * TILEB); \
    if ((i) < 7) { k4 = ld64(k4b + ((i) + 2) * C4B); k16 = ld128(kb + ((i) + 2) * TILEB); } SBAR(); \
    __builtin_amdgcn_s_setprio(0); \
    CVT4(pw0, SC, 0); EXP4(SC, 8); \
    o = MFMA32(v0f, BF8(pw0), o); SBAR(); \
    v0f = v1f; \
    EXP4(SC, 12); CVT4(pw1, SC, 8); \
    } while (0)
    ASTEP(0, sa, sb, cX); ASTEP(1, sb, sa, cX); ASTEP(2, sa, sb, cX); ASTEP(3, sb, sa, cX);
    ASTEP(4, sa, sb, cX); ASTEP(5, sb, sa, cX); ASTEP(6, sa, sb, cX); ASTEP(7, sb, sa, cD);
#undef ASTEP
    o = MFMA32(v1p, BF8(pw1), o);
    v1p = ld128(vb + 8 * TILEB + 1024);
    EXP4(sa, 0); EXP4(sa, 4); CVT4(pw0, sa, 0); EXP4(sa, 8); EXP4(sa, 12);
    o = MFMA32(v0f, BF8(pw0), o);
    CVT4(pw1, sa, 8);
    o = MFMA32(v1p, BF8(pw1), o);
    STAMP();
    if (__builtin_expect(acc_bad(o), 0)) slow_block<true>(ws + WS_KV + recA, max(Tq - 8, 0), Tq, Tq * 32 + r32, q16a, q4a, o, negm);
    store_o(o, ws + WS_O + (size_t)(Tq * 32) * (DM * 2) + h * 40, (lds_ptr)lds + A_END + 2 * SLOTB + wid * 1280);
  }
  STAMP_END();
}

constexpr int K3_XB = 128 * 128;
constexpr int K3_WGRP = 8 * 160 + 128;
constexpr int K3_WB = 8 * K3_WGRP;
constexpr int K3_STAGE = K3_XB + K3_WB;
constexpr int K3_NSLOT = 3;
constexpr int K3_LDS = K3_NSLOT * K3_STAGE + 256;

__global__ __launch_bounds__(512) void k3_out(const char* __restrict__ woA, const char* __restrict__ woB, const char* __restrict__ ws, unsigned short* __restrict__ out) {
  extern __shared__ __attribute__((aligned(16))) char lds[];
  const int lane = threadIdx.x & 63, wid = __builtin_amdgcn_readfirstlane(threadIdx.x >> 6);
  const int g = blockIdx.x & 7, i = blockIdx.x >> 3;
  STAMP_INIT(2, K3_LDS);
  const int br = g & 1, mt = (g >> 1) * 4 + (i & 3), j = i >> 2;
  const unsigned lds0 = (unsigned)(uintptr_t)lds;
  const int pl2 = min(max(lane - 16, 0), 19), pidx = i * 20 + pl2, prow = pidx / 10, pc = pidx - prow * 10;
  const char* pfp = lane < 16 ? ws + WS_O + (size_t)(br * SEQ + (g >> 1) * 512 + i * 16 + lane) * 1280 : (br ? woB : woA) + (size_t)prow * 1280 + pc * 128;
  const int pfs = lane < 16 ? 128 : 64 * 1280;
  if (wid >= 4) {
    const int lw = wid - 4;

    const __amdgpu_buffer_rsrc_t srdX = mksrd(ws + WS_O + (size_t)br * SEQ * DM * 2, SEQ * DM * 2), srdW = mksrd(br ? woB : woA, DM * DM * 2);
    unsigned vx[4], vw[3];
#pragma unroll
    for (int e = 0; e < 4; ++e) { const int u = (4 * lw + e) * 64 + lane, tok = u >> 3, ch = (u & 7) ^ (tok & 7); vx[e] = (mt * 128 + tok) * 1280 + ch * 16; }
#pragma unroll
    for (int e = 0; e < 3; ++e) {
      const int n = min(4 * e + lw, 10), u = n * 64 + lane, grp = u / 88, rem = u - grp * 88, r = rem / 10, ch = rem - r * 10;
      vw[e] = rem < 80 ? (grp * 8 + r) * 1280 + j * 160 + ch * 16 : OOB;
    }
    const int rot = ROT3 ? j + ((mt & 3) * 5 >> 1) : 0;
    auto issue = [&](int sq, int slot) {
      int st = sq + rot; st -= st >= 10 ? 10 : 0; st -= st >= 10 ? 10 : 0;
      const unsigned b = lds0 + slot * K3_STAGE;
      dma16(b + K3_XB + lw * 1024, vw[0], srdW, st * 64 * 1280);
      dma16(b + (4 * lw) * 1024, vx[0], srdX, st * 128);
      dma16(b + (4 * lw + 1) * 1024, vx[1], srdX, st * 128);
      dma16(b + K3_XB + (4 + lw) * 1024, vw[1], srdW, st * 64 * 1280);
      dma16(b + (4 * lw + 2) * 1024, vx[2], srdX, st * 128);
      dma16(b + (4 * lw + 3) * 1024, vx[3], srdX, st * 128);
      dma16(b + K3_XB + min(8 + lw, 10) * 1024, vw[2], srdW, st * 64 * 1280);
    };
    STAMP();
    issue(0, 0); issue(1, 1);
    int slot = 2;
#pragma nounroll
    for (int st = 0; st < 10; ++st) {
      if (st < 9) WAITBAR(7); else WAITBAR(0);
      STAMP();
      if (st < 8) issue(st + 2, slot);
      slot = slot == K3_NSLOT - 1 ? 0 : slot + 1;
    }
    asm volatile("s_barrier" ::: "memory");
    STAMP_END();
    return;
  }
  if (PF3 && wid >= 2) touch_line(lds0 + K3_LDS - 256, pfp + (size_t)wid * pfs);
  const int wt = wid, fr = lane & 15, gq = lane >> 4;
  unsigned xb[2][2], wb[5];
#pragma unroll
  for (int ks = 0; ks < 2; ++ks) { xb[ks][0] = lds0 + (wt * 32 + fr) * 128 + (((4 * ks + gq) ^ (fr & 7)) << 4); xb[ks][1] = xb[ks][0] + 2048; }
#pragma unroll
  for (int c = 0; c < 5; ++c) wb[c] = lds0 + K3_XB + gq * K3_WGRP + (fr >> 2) * 160 + (fr & 3) * 8 + c * 32;
  f32x4 acc[2][5];
#pragma unroll
  for (int a = 0; a < 2; ++a)
#pragma unroll
    for (int c = 0; c < 5; ++c) acc[a][c] = f32x4{0.f, 0.f, 0.f, 0.f};
  gemm_consume16<false, 4 * K3_WGRP, 4 * 160, K3_STAGE, K3_NSLOT>(acc, xb, wb, [&](int st) {
    if (st == 0) STAMP();
    if (PF3 && st < 6 && (st & 3) == wid) touch_line(lds0 + K3_LDS - 256, pfp + (size_t)(st + 4) * pfs);
  });
  STAMP();
  const lds_ptr stg = (lds_ptr)lds + wt * 5120;
#pragma unroll
  for (int a = 0; a < 2; ++a)
#pragma unroll
    for (int c = 0; c < 5; ++c)
      *(__attribute__((address_space(3))) u32x2*)(stg + (a * 16 + fr) * 160 + c * 32 + gq * 8) = u32x2{cvtpk(acc[a][c][0], acc[a][c][1]), cvtpk(acc[a][c][2], acc[a][c][3])};
#pragma unroll
  for (int e = 0; e < 5; ++e) {
    const int u = e * 64 + lane, tok = u / 10, c = u - tok * 10;
    *(u32x4*)(out + (size_t)(mt * 128 + wt * 32 + tok) * (2 * DM) + br * DM + j * 80 + c * 8) = *(const __attribute__((address_space(3))) u32x4*)(stg + u * 16);
  }
  STAMP_END();
}

extern "C" void kernel_launch(void* const* d_in, const int* in_sizes, int n_in, void* d_out, int out_size, void* d_ws, size_t ws_size, hipStream_t stream) {
  (void)in_sizes; (void)n_in; (void)out_size; (void)ws_size;
  (void)hipFuncSetAttribute((const void*)k1_qkv, hipFuncAttributeMaxDynamicSharedMemorySize, K1_LDS + STAMP_EXTRA);
  (void)hipFuncSetAttribute((const void*)k2_attn, hipFuncAttributeMaxDynamicSharedMemorySize, K2_LDS + STAMP_EXTRA);
  (void)hipFuncSetAttribute((const void*)k3_out, hipFuncAttributeMaxDynamicSharedMemorySize, K3_LDS + STAMP_EXTRA);
  char* ws = (char*)d_ws;
  for (int rep = 0; rep < REP1; ++rep)
  k1_qkv<<<256, 768, K1_LDS + STAMP_EXTRA, stream>>>((const char*)d_in[0], (const char*)d_in[1], (const char*)d_in[3], (const char*)d_in[4], ws, (const char*)d_in[2], (const char*)d_in[5], (const char*)d_out);
  for (int rep = 0; rep < REP2; ++rep)
  k2_attn<<<256, 576, K2_LDS + STAMP_EXTRA, stream>>>(ws);
  for (int rep = 0; rep < REP3; ++rep)
  k3_out<<<256, 512, K3_LDS + STAMP_EXTRA, stream>>>((const char*)d_in[2], (const char*)d_in[5], ws, (unsigned short*)d_out);
}
```

```cpp
#include <hip/hip_runtime.h>
#include <hip/hip_bf16.h>
#include <stdint.h>

typedef float f32x16 __attribute__((ext_vector_type(16)));
typedef float f32x4 __attribute__((ext_vector_type(4)));
typedef float f32x2 __attribute__((ext_vector_type(2)));
typedef short bf16x8 __attribute__((ext_vector_type(8)));
typedef short bf16x4 __attribute__((ext_vector_type(4)));
typedef unsigned u32x4 __attribute__((ext_vector_type(4)));
typedef unsigned u32x2 __attribute__((ext_vector_type(2)));
typedef __bf16 bf16x2_t __attribute__((ext_vector_type(2)));
typedef __attribute__((address_space(3))) const char* lds_cptr;
typedef __attribute__((address_space(3))) char* lds_ptr;

#define DI __device__ __forceinline__
#define SBAR() __builtin_amdgcn_sched_barrier(0)
#define PIN(x) asm volatile("" : "+v"(x))
#define MFMA32(a, b, c) __builtin_amdgcn_mfma_f32_32x32x16_bf16(a, b, c, 0, 0, 0)
#define MFMA32K8(a, b, c) __builtin_amdgcn_mfma_f32_32x32x8bf16_1k(a, b, c, 0, 0, 0)
#define MFMA16(a, b, c) __builtin_amdgcn_mfma_f32_16x16x32_bf16(a, b, c, 0, 0, 0)
#define EXP2(x) __builtin_amdgcn_exp2f(x)

#define GEXP 0
#define K0 0
#define K0REP 1
#define REP1 1
#define REP2 1
#define REP3 1
#define ROT1 0
#define ROT3 0
#define PFD 4
#define PFT 0
#define AUX1 16
#define AUX2 0
#define AUX3 0
#define PFW 1
#define PFC 1
#define PF1 0
#define PF3 0
constexpr int SEQ = 2048, DM = 640, NH = 32, N3 = 1920, NT = 64;
constexpr int QREC = 1280;
constexpr int KVREC = 2624;
constexpr int VOFF = 1280, VCH = 336;
constexpr size_t WS_Q = 0;
constexpr size_t WS_KV = (size_t)2 * NH * NT * QREC;
constexpr size_t WS_O = WS_KV + (size_t)2 * NH * NT * KVREC;
constexpr unsigned OOB = 0x40000000u;
constexpr float QSCALE = 1.4426950408889634f * 0.22360679774997896f;

DI unsigned cvtpk(float lo, float hi) { f32x2 v = {lo, hi}; bf16x2_t b = __builtin_convertvector(v, bf16x2_t); return __builtin_bit_cast(unsigned, b); }
DI bf16x8 ld128(lds_cptr p) { return *(const __attribute__((address_space(3))) bf16x8*)p; }
DI bf16x4 ld64(lds_cptr p) { return *(const __attribute__((address_space(3))) bf16x4*)p; }
DI bf16x4 ldtr(lds_cptr p) { return __builtin_bit_cast(bf16x4, __builtin_amdgcn_ds_read_tr16_b64_v4i16((__attribute__((address_space(3))) bf16x4*)p)); }
DI bf16x8 cat(bf16x4 a, bf16x4 b) { return __builtin_shufflevector(a, b, 0, 1, 2, 3, 4, 5, 6, 7); }
DI void dma16(unsigned m0v, unsigned voff, __amdgpu_buffer_rsrc_t srd, unsigned soff) {
  asm volatile("s_mov_b32 m0, %0\n\ts_nop 0\n\tbuffer_load_dwordx4 %1, %2, %3 offen lds" ::"s"(m0v), "v"(voff), "s"(srd), "s"(soff) : "memory");
}
DI bf16x8 gld128(const void* p) { bf16x8 r; asm volatile("global_load_dwordx4 %0, %1, off" : "=v"(r) : "v"(p) : "memory"); return r; }
DI bf16x4 gld64(const void* p) { bf16x4 r; asm volatile("global_load_dwordx2 %0, %1, off" : "=v"(r) : "v"(p) : "memory"); return r; }
DI void touch_line(unsigned m0v, const void* p) {
  asm volatile("s_mov_b32 m0, %0\n\ts_nop 0\n\tglobal_load_lds_dword %1, off" ::"s"(m0v), "v"(p) : "memory");
}
#define CODE_ADDR(SYM) ({ unsigned lo_, hi_; asm volatile("s_getpc_b64 s[40:41]\n\ts_add_u32 s40, s40, " SYM "@rel32@lo+4\n\ts_addc_u32 s41, s41, " SYM "@rel32@hi+12\n\ts_mov_b32 %0, s40\n\ts_mov_b32 %1, s41" \
  : "=s"(lo_), "=s"(hi_) :: "s40", "s41", "scc"); (const char*)(((unsigned long long)hi_ << 32) | lo_); })
constexpr int K1_CODE = 8192, K2_CODE = 13312, K3_CODE = 3584;
DI __amdgpu_buffer_rsrc_t mksrd(const void* p, unsigned bytes) { return __builtin_amdgcn_make_buffer_rsrc((void*)p, 0, bytes, 0x00020000); }
#define STAMP_EXTRA 0
#define STAMP_C()
#define STAMP_INIT(K, LDSOFF)
#define STAMP()
#define STAMP2()
#define STAMP_END()
#define WAVE_SYNC() do { __builtin_amdgcn_fence(__ATOMIC_RELEASE, "wavefront"); __builtin_amdgcn_wave_barrier(); __builtin_amdgcn_fence(__ATOMIC_ACQUIRE, "wavefront"); } while (0)
#define WAITBAR(N) asm volatile("s_waitcnt vmcnt(" #N ") lgkmcnt(0)\n\ts_barrier" ::: "memory")

constexpr int K1_XB = 128 * 128;
constexpr int K1_WB = 64 * 512;
constexpr int K1_STAGE = K1_XB + K1_WB;
constexpr int K1_LDS = 3 * K1_STAGE + 256;
DI int wswz(int k) { return ((k & 3) << 2) | (((k >> 3) & 1) << 1); }

#define LGKM(N) do { asm volatile("s_waitcnt lgkmcnt(" #N ")" ::: "memory"); SBAR(); } while (0)
template <int OFF> DI void rd_tr(bf16x4& r, unsigned a) { asm volatile("ds_read_b64_tr_b16 %0, %1 offset:%2" : "=v"(r) : "v"(a), "i"(OFF)); SBAR(); }
template <int OFF> DI void rd_128(bf16x8& r, unsigned a) { asm volatile("ds_read_b128 %0, %1 offset:%2" : "=v"(r) : "v"(a), "i"(OFF)); SBAR(); }
template <int G, bool VT, int KSO, int ROW4, class Hook>
DI void gemm_group16(f32x4 (&acc)[2][5], bf16x4 (&wl)[5], bf16x4 (&wh)[5], bf16x8 (&xf)[2][2], const unsigned (&w)[5], const unsigned (&wn)[5], unsigned xa, unsigned xb, int st, Hook&& hook) {
  constexpr bool LAST = G == 1;
  constexpr int WO = LAST ? 0 : KSO;
#define GM(c, a) do { if (VT) acc[a][c] = MFMA16(xf[G][a], cat(wl[c], wh[c]), acc[a][c]); else acc[a][c] = MFMA16(cat(wl[c], wh[c]), xf[G][a], acc[a][c]); SBAR(); } while (0)
#define RL(c) rd_tr<WO>(wl[c], LAST ? wn[c] : w[c])
#define RH(c) rd_tr<WO + ROW4>(wh[c], LAST ? wn[c] : w[c])
  LGKM(8);
  GM(0, 0);
  if (!LAST) rd_128<0>(xf[G ^ 1][0], xa);
  GM(0, 1);
  if (LAST) { asm volatile("s_waitcnt lgkmcnt(0)\n\ts_barrier" ::: "memory"); SBAR(); hook(st + 1); rd_128<0>(xf[G ^ 1][0], xa); }
  rd_128<0>(xf[G ^ 1][1], xb);
  RL(0);
  LGKM(9); GM(1, 0); RH(0); GM(1, 1); RL(1);
  LGKM(9); GM(2, 0); RH(1); GM(2, 1); RL(2);
  LGKM(9); GM(3, 0); RH(2); GM(3, 1); RL(3);
  LGKM(9); GM(4, 0); RH(3); GM(4, 1); RL(4); RH(4);
#undef GM
#undef RL
#undef RH
}
template <bool VT, int KSO, int ROW4, int STAGE, int NSLOT, class Hook>
DI void gemm_consume16(f32x4 (&acc)[2][5], const unsigned (&xb)[2][2], const unsigned (&wb)[5], Hook&& hook) {
  bf16x4 wl[5], wh[5]; bf16x8 xf[2][2];
  asm volatile("s_barrier" ::: "memory");
  hook(0);
  rd_128<0>(xf[0][0], xb[0][0]); rd_128<0>(xf[0][1], xb[0][1]);
#pragma unroll
  for (int c = 0; c < 5; ++c) { rd_tr<0>(wl[c], wb[c]); rd_tr<ROW4>(wh[c], wb[c]); }
  int so = 0;
#pragma nounroll
  for (int st = 0; st < 10; ++st) {
    const int son = so == (NSLOT - 1) * STAGE ? 0 : so + STAGE;
    unsigned w[5], wn[5];
#pragma unroll
    for (int c = 0; c < 5; ++c) { w[c] = wb[c] + so; wn[c] = wb[c] + son; }
    gemm_group16<0, VT, KSO, ROW4>(acc, wl, wh, xf, w, wn, xb[1][0] + so, xb[1][1] + so, st, hook);
    gemm_group16<1, VT, KSO, ROW4>(acc, wl, wh, xf, w, wn, xb[0][0] + son, xb[0][1] + son, st, hook);
    so = son;
  }
  LGKM(0);
}
template <int K, class Hook>
DI void gemm_step32(f32x16 (&acc)[5], bf16x4 (&wl)[5], bf16x4 (&wh)[5], bf16x8 (&xf)[2], const unsigned (&w)[5], const unsigned (&wn)[5], unsigned xnext, int st, Hook&& hook) {
  constexpr bool LAST = K == 3;
  constexpr int WO = LAST ? 0 : (K + 1) * 16 * 512;
#define GM(c) do { acc[c] = MFMA32(cat(wl[c], wh[c]), xf[K & 1], acc[c]); SBAR(); } while (0)
#define RW(c) do { rd_tr<WO>(wl[c], LAST ? wn[c] : w[c]); rd_tr<WO + 4 * 512>(wh[c], LAST ? wn[c] : w[c]); } while (0)
  LGKM(8);
  GM(0);
  if (LAST) { asm volatile("s_waitcnt lgkmcnt(0)\n\ts_barrier" ::: "memory"); SBAR(); hook(st + 1); }
  rd_128<0>(xf[(K & 1) ^ 1], xnext);
  RW(0);
  LGKM(9); GM(1); RW(1);
  LGKM(9); GM(2); RW(2);
  LGKM(9); GM(3); RW(3);
  LGKM(9); GM(4); RW(4);
#undef GM
#undef RW
}
template <int STAGE, int NSLOT, class Hook>
DI void gemm_consume32(f32x16 (&acc)[5], const unsigned (&xb)[4], const unsigned (&wb)[5], Hook&& hook) {
  bf16x4 wl[5], wh[5]; bf16x8 xf[2];
  asm volatile("s_barrier" ::: "memory");
  hook(0);
  rd_128<0>(xf[0], xb[0]);
#pragma unroll
  for (int c = 0; c < 5; ++c) { rd_tr<0>(wl[c], wb[c]); rd_tr<4 * 512>(wh[c], wb[c]); }
  int so = 0;
#pragma nounroll
  for (int st = 0; st < 10; ++st) {
    const int son = so == (NSLOT - 1) * STAGE ? 0 : so + STAGE;
    unsigned w[5], wn[5];
#pragma unroll
    for (int c = 0; c < 5; ++c) { w[c] = wb[c] + so; wn[c] = wb[c] + son; }
    gemm_step32<0>(acc, wl, wh, xf, w, wn, xb[1] + so, st, hook);
    gemm_step32<1>(acc, wl, wh, xf, w, wn, xb[2] + so, st, hook);
    gemm_step32<2>(acc, wl, wh, xf, w, wn, xb[3] + so, st, hook);
    gemm_step32<3>(acc, wl, wh, xf, w, wn, xb[0] + son, st, hook);
    so = son;
  }
  LGKM(0);
}

__global__ __launch_bounds__(768) void k1_qkv(const char* __restrict__ xA, const char* __restrict__ wA, const char* __restrict__ xB, const char* __restrict__ wB, char* __restrict__ ws,
                                              const char* __restrict__ woA, const char* __restrict__ woB, const char* __restrict__ outp) {
  extern __shared__ __attribute__((aligned(16))) char lds[];
  const int lane = threadIdx.x & 63, wid = __builtin_amdgcn_readfirstlane(threadIdx.x >> 6);
  STAMP_INIT(0, K1_LDS);
  const int g = blockIdx.x & 7, i = blockIdx.x >> 3;
  const int br = g & 1, mt = ((g >> 1) & 1) * 8 + (i & 7), j = (g >> 2) * 4 + (i >> 3);
  const unsigned lds0 = (unsigned)(uintptr_t)lds;
  if (wid >= 8) {
    const int lw = wid - 8;
    const __amdgpu_buffer_rsrc_t srdX = mksrd(br ? xB : xA, SEQ * DM * 2), srdW = mksrd(br ? wB : wA, DM * N3 * 2);
    unsigned vx[4], vw[8];
#pragma unroll
    for (int e = 0; e < 4; ++e) { const int u = (4 * lw + e) * 64 + lane, tok = u >> 3, ch = (u & 7) ^ ((tok >> 1) & 7); vx[e] = (mt * 128 + tok) * 1280 + ch * 16; }
#pragma unroll
    for (int e = 0; e < 8; ++e) {
      const int k = 2 * (4 * e + lw) + (lane >> 5), ch = (lane & 31) ^ wswz(k);
      const unsigned col = ch < 20 ? 320 * j + ch * 16 : 2560 + 160 * j + (ch - 20) * 16;
      vw[e] = ch < 30 ? k * 3840 + col : OOB;
    }
    const int rot = ROT1 == 1 ? (mt & 7) + ((j & 3) * 5 >> 1) : ROT1 == 2 ? 5 * ((mt + j) & 1) : ROT1 == 3 ? ((mt + j) & 3) * 5 >> 1 : 0;
    auto issue = [&](int sq, int slot) {
      int st = sq + rot; st -= st >= 10 ? 10 : 0; st -= st >= 10 ? 10 : 0;
      const unsigned b = lds0 + slot * K1_STAGE;
#pragma unroll
      for (int e = 0; e < 4; ++e) {
        dma16(b + (4 * lw + e) * 1024, vx[e], srdX, st * 128);
        dma16(b + K1_XB + (8 * e + lw) * 1024, vw[2 * e], srdW, st * 64 * 3840);
        dma16(b + K1_XB + (8 * e + 4 + lw) * 1024, vw[2 * e + 1], srdW, st * 64 * 3840);
      }
    };
    STAMP();
    issue(0, 0); issue(1, 1);
    STAMP();
    int slot = 0;
#pragma nounroll
    for (int st = 0; st < 10; ++st) {
      if (st < 9) WAITBAR(12); else WAITBAR(0);
      STAMP();
      if (st < 8) issue(st + 2, slot == 0 ? 2 : slot - 1);
      slot = slot == 2 ? 0 : slot + 1;
    }
    asm volatile("s_barrier" ::: "memory");
    STAMP_END();
    return;
  }
  if (wid == 0 && i < 4) {
    const int ln = i * 64 + lane;
    const char* c1 = CODE_ADDR("_Z6k1_qkvPKcS0_S0_S0_PcS0_S0_S0_"); const char* c2 = CODE_ADDR("_Z7k2_attnPcPKcS1_"); const char* c3 = CODE_ADDR("_Z6k3_outPKcS0_S0_Pc");
    constexpr int n1 = K1_CODE / 128, n2 = K2_CODE / 128, n3 = K3_CODE / 128;
    const char* p = ln < n2 ? c2 + ln * 128 : ln < n2 + n3 ? c3 + (ln - n2) * 128 : c1 + min(ln - n2 - n3, n1 - 1) * 128;
    touch_line(lds0 + K1_LDS - 256, p);
  }
  const int wt = wid & 3;
  const int tile = mt * 4 + wt;
  const int fr = lane & 15, gq = lane >> 4;
  if (wid < 4) {
    const int t32 = lane & 31, hi = lane >> 5;
    unsigned xb[4], wb[5];
#pragma unroll
    for (int k = 0; k < 4; ++k) xb[k] = lds0 + (wt * 32 + t32) * 128 + (((2 * k + hi) ^ ((t32 >> 1) & 7)) << 4);
    {
      const int k = 8 * hi + (fr >> 2), sw = wswz(k);
#pragma unroll
      for (int c = 0; c < 5; ++c) wb[c] = lds0 + K1_XB + k * 512 + (((4 * c + 2 * (gq & 1) + ((fr & 3) >> 1)) ^ sw) << 4) + (fr & 1) * 8;
    }
    unsigned wo[5][4];
#pragma unroll
    for (int c = 0; c < 5; ++c)
#pragma unroll
      for (int q = 0; q < 4; ++q) {
        const int f = 32 * c + 8 * q + 4 * hi, hs = (f * 205) >> 12, d0 = f - hs * 20;
        wo[c][q] = lds0 + wt * 10240 + hs * 1280 + (d0 < 16 ? (d0 >> 3) * 512 + t32 * 16 + (d0 & 7) * 2 : 1024 + t32 * 8);
        PIN(wo[c][q]);
      }
    const bool isq = j < 4;
    const int rec = isq ? QREC : KVREC;
    unsigned go[10];
#pragma unroll
    for (int e = 0; e < 10; ++e) { const int u = e * 64 + lane, hs = (u * 52429) >> 22; go[e] = hs * (NT * rec) + (u - hs * 80) * 16; PIN(go[e]); }
    const __amdgpu_buffer_rsrc_t srdO = mksrd(ws + (isq ? WS_Q : WS_KV), 2u * NH * NT * KVREC);
    const unsigned so = (unsigned)((br * NH + ((8 * j) & 31)) * NT + tile) * rec;
    f32x16 acc[5];
#pragma unroll
    for (int c = 0; c < 5; ++c) acc[c] = f32x16{};
    gemm_consume32<K1_STAGE, 3>(acc, xb, wb, [&](int st) { if (st == 0) STAMP(); });
    STAMP();
    if (isq) {
#pragma unroll
      for (int c = 0; c < 5; ++c) acc[c] *= QSCALE;
    }
    const unsigned sr = lds0 + wt * 10240 + lane * 16;
#pragma unroll
    for (int half = 0; half < 2; ++half) {
#pragma unroll
      for (int c = 0; c < 5; ++c)
#pragma unroll
        for (int q = 0; q < 4; ++q) {
          const int fq = 32 * c + 8 * q;
          if ((fq < 80) == (half == 0))
            *(__attribute__((address_space(3))) u32x2*)(uintptr_t)wo[c][q] = u32x2{cvtpk(acc[c][4 * q], acc[c][4 * q + 1]), cvtpk(acc[c][4 * q + 2], acc[c][4 * q + 3])};
        }
      WAVE_SYNC();
      u32x4 v[5];
#pragma unroll
      for (int e = 0; e < 5; ++e) v[e] = *(const __attribute__((address_space(3))) u32x4*)(uintptr_t)(sr + (half * 5 + e) * 1024);
#pragma unroll
      for (int e = 0; e < 5; ++e) __builtin_amdgcn_raw_buffer_store_b128(v[e], srdO, go[half * 5 + e], so, AUX1);
      SBAR();
    }
  } else {
    const int tok0 = wt * 32 + 16 * (fr >> 3) + (fr & 7);
    unsigned xb[2][2], wb[5];
#pragma unroll
    for (int ks = 0; ks < 2; ++ks)
#pragma unroll
      for (int a = 0; a < 2; ++a) { const int tok = tok0 + 8 * a; xb[ks][a] = lds0 + tok * 128 + (((4 * ks + gq) ^ ((tok >> 1) & 7)) << 4); }
    {
      const int k = 8 * gq + (fr >> 2), sw = wswz(k);
#pragma unroll
      for (int c = 0; c < 5; ++c) wb[c] = lds0 + K1_XB + k * 512 + (((20 + 2 * c + ((fr & 3) >> 1)) ^ sw) << 4) + (fr & 1) * 8;
    }
    unsigned vo[5];
#pragma unroll
    for (int c = 0; c < 5; ++c) { const int f = c * 16 + fr, hs = (f * 205) >> 12, d = f - hs * 20; vo[c] = hs * (NT * KVREC) + VOFF + gq * VCH + d * 16; PIN(vo[c]); }
    f32x4 acc[2][5];
#pragma unroll
    for (int a = 0; a < 2; ++a)
#pragma unroll
      for (int c = 0; c < 5; ++c) acc[a][c] = f32x4{0.f, 0.f, 0.f, 0.f};
    gemm_consume16<true, 32 * 512, 4 * 512, K1_STAGE, 3>(acc, xb, wb, [&](int st) { if (st == 0) STAMP(); });
    STAMP();
    const __amdgpu_buffer_rsrc_t srdO = mksrd(ws + WS_KV, 2u * NH * NT * KVREC);
    const unsigned so = (unsigned)((br * NH + 4 * j) * NT + tile) * KVREC;
#pragma unroll
    for (int c = 0; c < 5; ++c) {
      const u32x4 v = {cvtpk(acc[0][c][0], acc[0][c][1]), cvtpk(acc[0][c][2], acc[0][c][3]), cvtpk(acc[1][c][0], acc[1][c][1]), cvtpk(acc[1][c][2], acc[1][c][3])};
      __builtin_amdgcn_raw_buffer_store_b128(v, srdO, vo[c], so, AUX1);
    }
    if (lane < 16) __builtin_amdgcn_raw_buffer_store_b128(u32x4{0x3f803f80u, 0x3f803f80u, 0x3f803f80u, 0x3f803f80u}, srdO, (lane >> 2) * (NT * KVREC) + VOFF + (lane & 3) * VCH + 320, so, AUX1);
  }
  STAMP_END();
}

constexpr int TILEB = 3072;
constexpr int C4B = 512;
constexpr int A_MAIN = 0, A_C4 = 16 * TILEB, A_Q = A_C4 + 16 * C4B, A_END = A_Q + 8 * QREC;
constexpr int SLOT_C4 = 8 * TILEB, SLOTB = SLOT_C4 + 8 * C4B;
constexpr int K2_LDS = A_END + 3 * SLOTB + 256;
constexpr float NEGINF = -__builtin_huge_valf();

DI int crow(int r, int hi) { return (r & 3) + 8 * (r >> 2) + 4 * hi; }
DI float rowmax16(const f32x16& s) {
  float m = fmaxf(fmaxf(s[0], s[1]), s[2]);
#pragma unroll
  for (int r = 3; r < 15; r += 2) m = fmaxf(fmaxf(m, s[r]), s[r + 1]);
  m = fmaxf(m, s[15]);
  auto rr = __builtin_amdgcn_permlane32_swap(__float_as_uint(m), __float_as_uint(m), false, false);
  return fmaxf(__uint_as_float(rr[0]), __uint_as_float(rr[1]));
}
DI bool acc_bad(const f32x16& o) {
  float t = 0.f;
#pragma unroll
  for (int r = 0; r < 12; ++r) t = __builtin_fmaf(o[r], 0.f, t);
  const bool bad = !(t == 0.f) || !(o[8] < 1.0e36f);
  return __any(bad);
}
template <bool WINDOW>
DI void slow_block(const char* __restrict__ kvh, int t_lo, int t_hi, int qrow, bf16x8 q16, bf16x4 q4, f32x16& o, float& negm) {
  const int lane = threadIdx.x & 63, r32 = lane & 31, hi = lane >> 5;
  float m = NEGINF;
  for (int pass = 0; pass < 2; ++pass) {
    for (int t = t_lo; t <= t_hi; ++t) {
      const char* rp = kvh + (size_t)t * KVREC;
      const bf16x8 k16 = *(const bf16x8*)(rp + hi * 512 + r32 * 16);
      const bf16x4 k4 = *(const bf16x4*)(rp + 1024 + r32 * 8);
      f32x16 s = {};
      s = MFMA32(k16, q16, s); s = MFMA32K8(k4, q4, s);
      if (WINDOW) {
#pragma unroll
        for (int r = 0; r < 16; ++r) { const int df = qrow - (t * 32 + crow(r, hi)); if (df < 0 || df >= 256) s[r] = NEGINF; }
      }
      if (pass == 0) {
#pragma unroll
        for (int r = 0; r < 16; ++r) m = fmaxf(m, s[r]);
      } else {
        const int vr = min(r32, 20);
        bf16x8 v0 = *(const bf16x8*)(rp + VOFF + hi * VCH + vr * 16), v1 = *(const bf16x8*)(rp + VOFF + (2 + hi) * VCH + vr * 16);
        u32x4 p0, p1;
#pragma unroll
        for (int r = 0; r < 4; ++r) { p0[r] = cvtpk(EXP2(s[2 * r] - m), EXP2(s[2 * r + 1] - m)); p1[r] = cvtpk(EXP2(s[8 + 2 * r] - m), EXP2(s[9 + 2 * r] - m)); }
        o = MFMA32(v0, __builtin_bit_cast(bf16x8, p0), o); o = MFMA32(v1, __builtin_bit_cast(bf16x8, p1), o);
      }
    }
    if (pass == 0) {
      auto rr = __builtin_amdgcn_permlane32_swap(__float_as_uint(m), __float_as_uint(m), false, false);
      m = fmaxf(__uint_as_float(rr[0]), __uint_as_float(rr[1]));
      o = f32x16{};
    }
  }
  negm = -m;
}
DI void store_o(const f32x16& o, __amdgpu_buffer_rsrc_t srd, unsigned so, lds_ptr stg) {
  const int lane = threadIdx.x & 63, r32 = lane & 31, hi = lane >> 5;
  auto rr = __builtin_amdgcn_permlane32_swap(__float_as_uint(o[8]), __float_as_uint(o[8]), false, false);
  const float inv = __builtin_amdgcn_rcpf(__uint_as_float(rr[1]));
  typedef __attribute__((address_space(3))) u32x2* lds_u2;
  *(lds_u2)(stg + r32 * 40 + 8 * hi) = u32x2{cvtpk(o[0] * inv, o[1] * inv), cvtpk(o[2] * inv, o[3] * inv)};
  *(lds_u2)(stg + r32 * 40 + 16 + 8 * hi) = u32x2{cvtpk(o[4] * inv, o[5] * inv), cvtpk(o[6] * inv, o[7] * inv)};
  if (!hi) *(lds_u2)(stg + r32 * 40 + 32) = u32x2{cvtpk(o[8] * inv, o[9] * inv), cvtpk(o[10] * inv, o[11] * inv)};
  WAVE_SYNC();
#pragma unroll
  for (int e = 0; e < 3; ++e) {
    const int p = e * 64 + lane, row = (p * 52429) >> 18;
    if (e < 2 || lane < 32) __builtin_amdgcn_raw_buffer_store_b64(*(lds_u2)(stg + p * 8), srd, row * 1280 + (p - row * 5) * 8, so, AUX2);
  }
  WAVE_SYNC();
}
constexpr unsigned long long diag_mask(int r) {
  unsigned long long m = 0;
  for (int l = 0; l < 64; ++l) if ((r & 3) + 8 * (r >> 2) + 4 * (l >> 5) <= (l & 31)) m |= 1ull << l;
  return m;
}
#define EXP4(S_, i) do { S_[i] = EXP2(S_[i]); S_[i + 1] = EXP2(S_[i + 1]); S_[i + 2] = EXP2(S_[i + 2]); S_[i + 3] = EXP2(S_[i + 3]); PIN(S_); SBAR(); } while (0)
#define CVT4(P_, S_, i) do { P_[0] = cvtpk(S_[i], S_[i + 1]); P_[1] = cvtpk(S_[i + 2], S_[i + 3]); P_[2] = cvtpk(S_[i + 4], S_[i + 5]); P_[3] = cvtpk(S_[i + 6], S_[i + 7]); PIN(P_); SBAR(); } while (0)
#define BF8(P_) __builtin_bit_cast(bf16x8, P_)

__global__ __launch_bounds__(576) void k2_attn(char* __restrict__ ws, const char* __restrict__ woA, const char* __restrict__ woB) {
  extern __shared__ __attribute__((aligned(16))) char lds[];
  const int lane = threadIdx.x & 63, r32 = lane & 31, hi = lane >> 5;
  const int wid = __builtin_amdgcn_readfirstlane(threadIdx.x >> 6);
  const int g = blockIdx.x & 7, i = blockIdx.x >> 3;
  STAMP_INIT(1, K2_LDS);
  const int h = g * 4 + (i >> 3), T0 = (i & 7) * 8;
  const unsigned recA = (unsigned)h * NT * KVREC, recB = (unsigned)(NH + h) * NT * KVREC;
  if (wid == 8) {
    const unsigned lds0 = (unsigned)(uintptr_t)lds;
    const __amdgpu_buffer_rsrc_t srd = mksrd(ws + WS_KV, 2u * NH * NT * KVREC), srdQ = mksrd(ws + WS_Q, 2u * NH * NT * QREC);
    const unsigned v0 = lane * 16;
    const unsigned v1 = r32 < 21 ? VOFF + hi * VCH + r32 * 16 : OOB, v2 = r32 < 21 ? VOFF + (2 + hi) * VCH + r32 * 16 : OOB;
    const unsigned v3 = r32 < 16 ? hi * KVREC + 1024 + r32 * 16 : OOB;
    auto tile2 = [&](unsigned b, unsigned c4, unsigned so) {
      dma16(b, v0, srd, so); dma16(b + TILEB, v0, srd, so + KVREC); dma16(c4, v3, srd, so);
      dma16(b + 1024, v1, srd, so); dma16(b + 2048, v2, srd, so);
      dma16(b + TILEB + 1024, v1, srd, so + KVREC); dma16(b + TILEB + 2048, v2, srd, so + KVREC);
    };
    auto issueB = [&](int st, int slot) {
      const unsigned b = lds0 + A_END + slot * SLOTB;
#pragma unroll
      for (int p = 0; p < 4; ++p) tile2(b + 2 * p * TILEB, b + SLOT_C4 + 2 * p * C4B, recB + ((p >> 1) * 32 + 4 * st + 2 * (p & 1)) * KVREC);
    };
    auto issueQ = [&](unsigned dst, unsigned so) {
#pragma unroll
      for (int e = 0; e < 10; ++e) dma16(dst + e * 1024, v0, srdQ, so + e * 1024);
    };
    asm volatile("s_barrier" ::: "memory");
    STAMP();
    issueB(1, 1);
    int slot = 0;
#pragma nounroll
    for (int st = 0; st < 8; ++st) {
      const int nslot = slot == 2 ? 0 : slot + 1;
      WAITBAR(0);
      if (st < 6) issueB(st + 2, nslot == 2 ? 0 : nslot + 1);
      tile2(lds0 + A_MAIN + 2 * st * TILEB, lds0 + A_C4 + 2 * st * C4B, recA + max(T0 - 8 + 2 * st, 0) * KVREC);
      if (st == 6) issueQ(lds0 + A_Q, (h * NT + T0) * QREC);
      if (PFW && st == 5) {
        const char* wo = (g & 1) ? woB : woA;
#pragma unroll
        for (int e = 0; e < 4; ++e) {
          const int ln = e * 64 + lane;
          const char* p = wo + (size_t)(i * 200 + min(ln, 199)) * 128;
          if (e == 3 && i == 0 && lane >= 8 && lane < 8 + K3_CODE / 128) p = CODE_ADDR("_Z6k3_outPKcS0_S0_Pc") + (lane - 8) * 128;
          touch_line(lds0 + K2_LDS - 256, p);
        }
      }
      slot = nslot;
    }
    WAITBAR(0);
    STAMP_END();
    return;
  }
  {
    const int wr = wid & 3, ks = wid >> 2;
    {
      const unsigned lds0 = (unsigned)(uintptr_t)lds;
      const __amdgpu_buffer_rsrc_t srd = mksrd(ws + WS_KV, 2u * NH * NT * KVREC), srdQ = mksrd(ws + WS_Q, 2u * NH * NT * QREC);
      const unsigned so = recB + (ks * 32 + wr) * KVREC, b = lds0 + A_END + wid * TILEB;
      dma16(b, lane * 16, srd, so);
      if (!(wid & 1)) dma16(lds0 + A_END + SLOT_C4 + wid * C4B, r32 < 16 ? hi * KVREC + 1024 + r32 * 16 : OOB, srd, so);
      dma16(lds0 + A_MAIN + wid * 1024, lane * 16, srdQ, ((NH + h) * NT + T0) * QREC + wid * 1024);
      if (wid < 2) dma16(lds0 + A_MAIN + (8 + wid) * 1024, lane * 16, srdQ, ((NH + h) * NT + T0) * QREC + (8 + wid) * 1024);
      dma16(b + 1024, r32 < 21 ? VOFF + hi * VCH + r32 * 16 : OOB, srd, so);
      dma16(b + 2048, r32 < 21 ? VOFF + (2 + hi) * VCH + r32 * 16 : OOB, srd, so);
      asm volatile("s_waitcnt vmcnt(0)" ::: "memory");
    }
    const lds_cptr kb0 = (lds_cptr)lds + A_END + ks * 4 * TILEB + hi * 512 + r32 * 16;
    const lds_cptr k4b0 = (lds_cptr)lds + A_END + SLOT_C4 + ks * 4 * C4B + hi * 256 + r32 * 8;
    const lds_cptr vb0 = kb0 + 1024;
    asm volatile("s_barrier" ::: "memory");
    STAMP();
    bf16x8 q16[2]; bf16x4 q4[2];
#pragma unroll
    for (int b = 0; b < 2; ++b) {
      const lds_cptr qp = (lds_cptr)lds + A_MAIN + (2 * wr + b) * QREC;
      q16[b] = ld128(qp + hi * 512 + r32 * 16);
      q4[b] = ld64(qp + 1024 + r32 * 8);
      if (hi) q4[b] = bf16x4{0, 0, 0, 0};
    }
    f32x16 negm[2], o[2], sa, sb;
    bf16x8 k16[2], v0f[2], v1f[2]; bf16x4 k4[2];
    u32x4 pw0[2], pw1[2];
    k16[0] = ld128(kb0); k4[0] = ld64(k4b0);
#pragma unroll
    for (int b = 0; b < 2; ++b) {
      f32x16 s = {};
      s = MFMA32(k16[0], q16[b], s); s = MFMA32K8(k4[0], q4[b], s);
      const float nm = -rowmax16(s);
#pragma unroll
      for (int r = 0; r < 16; ++r) negm[b][r] = nm;
      PIN(negm[b]);
      o[b] = f32x16{};
    }
    sa = MFMA32(k16[0], q16[0], negm[0]); sa = MFMA32K8(k4[0], q4[0], sa);
    v0f[0] = ld128(vb0); v1f[0] = ld128(vb0 + 1024);
    pw0[1] = u32x4{0, 0, 0, 0}; pw1[1] = u32x4{0, 0, 0, 0}; v0f[1] = v0f[0]; v1f[1] = v1f[0];
    int slot = 0;
#pragma nounroll
    for (int st = 0; st < 8; ++st) {
      STAMP();
      const int nslot = slot == 2 ? 0 : slot + 1;
      const int off = slot * SLOTB, noff = nslot * SLOTB;
      const lds_cptr kb = kb0 + off, k4b = k4b0 + off, vb = vb0 + off;
      const lds_cptr kbn = kb0 + noff, k4bn = k4b0 + noff, vbn = vb0 + noff;
#define BSTEP(i) do { \
      constexpr int cb = (i) & 1, nb = cb ^ 1; \
      if ((i) == 3) { asm volatile("s_waitcnt lgkmcnt(0)\n\ts_barrier" ::: "memory"); SBAR(); } \
      const lds_cptr pk = (i) < 3 ? kb + ((i) + 1) * TILEB : kbn, pk4 = (i) < 3 ? k4b + ((i) + 1) * C4B : k4bn, pv = (i) < 3 ? vb + ((i) + 1) * TILEB : vbn; \
      __builtin_amdgcn_s_setprio(1); \
      sb = MFMA32(k16[cb], q16[1], negm[1]); SBAR(); \
      EXP4(sa, 0); \
      o[1] = MFMA32(v0f[nb], BF8(pw0[1]), o[1]); SBAR(); \
      k4[nb] = ld64(pk4); k16[nb] = ld128(pk); SBAR(); \
      EXP4(sa, 4); \
      sb = MFMA32K8(k4[cb], q4[1], sb); SBAR(); \
      CVT4(pw0[0], sa, 0); EXP4(sa, 8); \
      o[1] = MFMA32(v1f[nb], BF8(pw1[1]), o[1]); SBAR(); \
      v1f[nb] = ld128(pv + 1024); v0f[nb] = ld128(pv); SBAR(); \
      EXP4(sa, 12); CVT4(pw1[0], sa, 8); \
      __builtin_amdgcn_s_setprio(0); \
      sa = MFMA32(k16[nb], q16[0], negm[0]); SBAR(); \
      EXP4(sb, 0); \
      o[0] = MFMA32(v0f[cb], BF8(pw0[0]), o[0]); SBAR(); \
      EXP4(sb, 4); \
      sa = MFMA32K8(k4[nb], q4[0], sa); SBAR(); \
      CVT4(pw0[1], sb, 0); EXP4(sb, 8); \
      o[0] = MFMA32(v1f[cb], BF8(pw1[0]), o[0]); SBAR(); \
      EXP4(sb, 12); CVT4(pw1[1], sb, 8); \
      } while (0)
      BSTEP(0); BSTEP(1); BSTEP(2); BSTEP(3);
#undef BSTEP
      slot = nslot;
    }
    o[1] = MFMA32(v0f[1], BF8(pw0[1]), o[1]);
    o[1] = MFMA32(v1f[1], BF8(pw1[1]), o[1]);
    STAMP();
    float nm[2] = {negm[0][0], negm[1][0]};
#pragma unroll
    for (int b = 0; b < 2; ++b)
      if (__builtin_expect(acc_bad(o[b]), 0)) {
        slow_block<false>(ws + WS_KV + recB, ks * 32, ks * 32 + 31, 0, q16[b], q4[b], o[b], nm[b]);
      }
    typedef __attribute__((address_space(3))) f32x4* lds_f4;
    typedef __attribute__((address_space(3))) float* lds_f;
    const lds_ptr mb = (lds_ptr)lds + A_END + wr * 2 * 3328 + lane * 16, mn = (lds_ptr)lds + A_END + wr * 2 * 3328 + 3072 + lane * 4;
    if (ks == 1) {
#pragma unroll
      for (int b = 0; b < 2; ++b) {
        *(lds_f4)(mb + b * 3328) = f32x4{o[b][0], o[b][1], o[b][2], o[b][3]};
        *(lds_f4)(mb + b * 3328 + 1024) = f32x4{o[b][4], o[b][5], o[b][6], o[b][7]};
        *(lds_f4)(mb + b * 3328 + 2048) = f32x4{o[b][8], o[b][9], o[b][10], o[b][11]};
        *(lds_f)(mn + b * 3328) = nm[b];
      }
    }
    asm volatile("s_waitcnt lgkmcnt(0)\n\ts_barrier" ::: "memory");
    if (ks == 0) {
#pragma unroll
      for (int b = 0; b < 2; ++b) {
        const f32x4 p0 = *(lds_f4)(mb + b * 3328), p1 = *(lds_f4)(mb + b * 3328 + 1024), p2 = *(lds_f4)(mb + b * 3328 + 2048);
        const float n1 = *(lds_f)(mn + b * 3328), nx = fminf(nm[b], n1);
        const float f0 = EXP2(nx - nm[b]), f1 = EXP2(nx - n1);
#pragma unroll
        for (int r = 0; r < 4; ++r) { o[b][r] = o[b][r] * f0 + p0[r] * f1; o[b][4 + r] = o[b][4 + r] * f0 + p1[r] * f1; o[b][8 + r] = o[b][8 + r] * f0 + p2[r] * f1; }
        store_o(o[b], mksrd(ws + WS_O, 2u * SEQ * DM * 2), (unsigned)(SEQ + (T0 + 2 * wr + b) * 32) * (DM * 2) + h * 40, (lds_ptr)lds + A_END + 2 * SLOTB + wid * 1280);
      }
    }
  }
  STAMP();
  {
    const int Tq = T0 + wid;
    const int i0 = max(8 - Tq, 0);
    const lds_cptr kb = (lds_cptr)lds + A_MAIN + wid * TILEB + hi * 512 + r32 * 16;
    const lds_cptr k4b = (lds_cptr)lds + A_C4 + wid * C4B + hi * 256 + r32 * 8;
    const lds_cptr vb = (lds_cptr)lds + A_MAIN + wid * TILEB + 1024 + hi * 512 + r32 * 16;
    const bf16x8 q16a = ld128((lds_cptr)lds + A_Q + wid * QREC + hi * 512 + r32 * 16);
    bf16x4 q4a = ld64((lds_cptr)lds + A_Q + wid * QREC + 1024 + r32 * 8);
    if (hi) q4a = bf16x4{0, 0, 0, 0};
    f32x16 cX, cF, cD;
    float negm;
    {
      const int ts = Tq ? 7 : 8;
      f32x16 s = {};
      s = MFMA32(ld128(kb + ts * TILEB), q16a, s); s = MFMA32K8(ld64(k4b + ts * C4B), q4a, s);
      if (__builtin_expect(Tq == 0, 0)) {
#pragma unroll
        for (int r = 0; r < 16; ++r) if (crow(r, hi) > r32) s[r] = NEGINF;
      }
      negm = -rowmax16(s);
      const float nF = i0 > 0 ? NEGINF : negm, nX = i0 > 1 ? NEGINF : negm;
#define MK(r) do { const bool dv = __builtin_amdgcn_inverse_ballot_w64(diag_mask(r)); cD[r] = dv ? negm : NEGINF; cF[r] = dv ? NEGINF : nF; cX[r] = nX; } while (0)
      MK(0); MK(1); MK(2); MK(3); MK(4); MK(5); MK(6); MK(7); MK(8); MK(9); MK(10); MK(11); MK(12); MK(13); MK(14); MK(15);
#undef MK
      PIN(cX); PIN(cF); PIN(cD);
    }
    f32x16 o = {}, sa, sb;
    bf16x8 k16, v0f, v1f, v1p; bf16x4 k4;
    u32x4 pw0, pw1 = {0, 0, 0, 0};
    sa = MFMA32(ld128(kb), q16a, cF); sa = MFMA32K8(ld64(k4b), q4a, sa);
    k16 = ld128(kb + TILEB); k4 = ld64(k4b + C4B); v0f = ld128(vb); v1p = ld128(vb + 1024);
#define ASTEP(i, SC, SN, CN) do { \
    if ((i) >= 1 && (i) <= 6) { if (__builtin_expect(i0 == (i) + 1, 0)) { _Pragma("unroll") for (int r = 0; r < 16; ++r) cX[r] = negm; PIN(cX); } } \
    __builtin_amdgcn_s_setprio(1); \
    SN = MFMA32(k16, q16a, CN); SBAR(); \
    EXP4(SC, 0); \
    o = MFMA32(v1p, BF8(pw1), o); SBAR(); \
    EXP4(SC, 4); \
    SN = MFMA32K8(k4, q4a, SN); SBAR(); \
    v1p = ld128(vb + (i) * TILEB + 1024); v1f = ld128(vb + ((i) + 1) * TILEB); \
    if ((i) < 7) { k4 = ld64(k4b + ((i) + 2) * C4B); k16 = ld128(kb + ((i) + 2) * TILEB); } SBAR(); \
    __builtin_amdgcn_s_setprio(0); \
    CVT4(pw0, SC, 0); EXP4(SC, 8); \
    o = MFMA32(v0f, BF8(pw0), o); SBAR(); \
    v0f = v1f; \
    EXP4(SC, 12); CVT4(pw1, SC, 8); \
    } while (0)
    ASTEP(0, sa, sb, cX); ASTEP(1, sb, sa, cX); ASTEP(2, sa, sb, cX); ASTEP(3, sb, sa, cX);
    ASTEP(4, sa, sb, cX); ASTEP(5, sb, sa, cX); ASTEP(6, sa, sb, cX); ASTEP(7, sb, sa, cD);
#undef ASTEP
    o = MFMA32(v1p, BF8(pw1), o);
    v1p = ld128(vb + 8 * TILEB + 1024);
    EXP4(sa, 0); EXP4(sa, 4); CVT4(pw0, sa, 0); EXP4(sa, 8); EXP4(sa, 12);
    o = MFMA32(v0f, BF8(pw0), o);
    CVT4(pw1, sa, 8);
    o = MFMA32(v1p, BF8(pw1), o);
    STAMP();
    if (__builtin_expect(acc_bad(o), 0)) slow_block<true>(ws + WS_KV + recA, max(Tq - 8, 0), Tq, Tq * 32 + r32, q16a, q4a, o, negm);
    store_o(o, mksrd(ws + WS_O, 2u * SEQ * DM * 2), (unsigned)(Tq * 32) * (DM * 2) + h * 40, (lds_ptr)lds + A_END + 2 * SLOTB + wid * 1280);
  }
  STAMP_END();
}

constexpr int K3_XB = 128 * 128;
constexpr int K3_WGRP = 8 * 160 + 128;
constexpr int K3_WB = 8 * K3_WGRP;
constexpr int K3_STAGE = K3_XB + K3_WB;
constexpr int K3_NSLOT = 5;
constexpr int K3_LDS = K3_NSLOT * K3_STAGE + 256;

__global__ __launch_bounds__(512) void k3_out(const char* __restrict__ woA, const char* __restrict__ woB, const char* __restrict__ ws, char* __restrict__ out) {
  extern __shared__ __attribute__((aligned(16))) char lds[];
  const int lane = threadIdx.x & 63, wid = __builtin_amdgcn_readfirstlane(threadIdx.x >> 6);
  const int g = blockIdx.x & 7, i = blockIdx.x >> 3;
  STAMP_INIT(2, K3_LDS);
  const int br = g & 1, mt = (g >> 1) * 4 + (i & 3), j = i >> 2;
  const unsigned lds0 = (unsigned)(uintptr_t)lds;
  if (wid >= 4) {
    const int lw = wid - 4;
    const __amdgpu_buffer_rsrc_t srdX = mksrd(ws + WS_O + (size_t)br * SEQ * DM * 2, SEQ * DM * 2), srdW = mksrd(br ? woB : woA, DM * DM * 2);
    unsigned vx[4], vw[3];
#pragma unroll
    for (int e = 0; e < 4; ++e) { const int u = (4 * lw + e) * 64 + lane, tok = u >> 3, ch = (u & 7) ^ (tok & 7); vx[e] = (mt * 128 + tok) * 1280 + ch * 16; }
#pragma unroll
    for (int e = 0; e < 3; ++e) {
      const int n = min(4 * e + lw, 10), u = n * 64 + lane, grp = (u * 745) >> 16, rem = u - grp * 88, r = (rem * 205) >> 11, ch = rem - r * 10;
      vw[e] = rem < 80 ? (grp * 8 + r) * 1280 + j * 160 + ch * 16 : OOB;
    }
    const int rot = ROT3 == 1 ? j + ((mt & 3) * 5 >> 1) : ROT3 == 2 ? 5 * ((mt + j) & 1) : ROT3 == 3 ? ((mt + j) & 3) * 5 >> 1 : 0;
    auto issue = [&](int sq, int slot) {
      int st = sq + rot; st -= st >= 10 ? 10 : 0; st -= st >= 10 ? 10 : 0;
      const unsigned b = lds0 + slot * K3_STAGE;
      dma16(b + (4 * lw) * 1024, vx[0], srdX, st * 128);
      dma16(b + K3_XB + lw * 1024, vw[0], srdW, st * 64 * 1280);
      dma16(b + (4 * lw + 1) * 1024, vx[1], srdX, st * 128);
      dma16(b + K3_XB + (4 + lw) * 1024, vw[1], srdW, st * 64 * 1280);
      dma16(b + (4 * lw + 2) * 1024, vx[2], srdX, st * 128);
      dma16(b + K3_XB + min(8 + lw, 10) * 1024, vw[2], srdW, st * 64 * 1280);
      dma16(b + (4 * lw + 3) * 1024, vx[3], srdX, st * 128);
    };
    STAMP();
    issue(0, 0); issue(1, 1);
    WAITBAR(7);
    STAMP();
    issue(2, 2); issue(3, 3);
    int slot = 4;
#pragma nounroll
    for (int st = 1; st < 10; ++st) {
      if (st < 8) WAITBAR(14); else if (st == 8) WAITBAR(7); else WAITBAR(0);
      STAMP();
      if (st < 7) issue(st + 3, slot);
      STAMP2();
      slot = slot == K3_NSLOT - 1 ? 0 : slot + 1;
    }
    asm volatile("s_barrier" ::: "memory");
    STAMP_END();
    return;
  }
  const int wt = wid, fr = lane & 15, gq = lane >> 4;
  unsigned xb[2][2], wb[5];
#pragma unroll
  for (int ks = 0; ks < 2; ++ks) { xb[ks][0] = lds0 + (wt * 32 + fr) * 128 + (((4 * ks + gq) ^ (fr & 7)) << 4); xb[ks][1] = xb[ks][0] + 2048; }
#pragma unroll
  for (int c = 0; c < 5; ++c) wb[c] = lds0 + K3_XB + gq * K3_WGRP + (fr >> 2) * 160 + (fr & 3) * 8 + c * 32;
  unsigned oo[5];
#pragma unroll
  for (int e = 0; e < 5; ++e) { const int u = e * 64 + lane, tok = (u * 205) >> 11; oo[e] = (mt * 128 + wt * 32 + tok) * (4 * DM) + br * (2 * DM) + j * 160 + (u - tok * 10) * 16; PIN(oo[e]); }
  unsigned sw = lds0 + wt * 5120 + fr * 160 + gq * 8, sr = lds0 + wt * 5120 + lane * 16;
  PIN(sw); PIN(sr);
  const __amdgpu_buffer_rsrc_t srdO = mksrd(out, SEQ * 2 * DM * 2);
  f32x4 acc[2][5];
#pragma unroll
  for (int a = 0; a < 2; ++a)
#pragma unroll
    for (int c = 0; c < 5; ++c) acc[a][c] = f32x4{0.f, 0.f, 0.f, 0.f};
  gemm_consume16<false, 4 * K3_WGRP, 4 * 160, K3_STAGE, K3_NSLOT>(acc, xb, wb, [&](int st) { if (st == 0) STAMP(); });
  STAMP();
#pragma unroll
  for (int a = 0; a < 2; ++a)
#pragma unroll
    for (int c = 0; c < 5; ++c)
      *(__attribute__((address_space(3))) u32x2*)(uintptr_t)(sw + a * 2560 + c * 32) = u32x2{cvtpk(acc[a][c][0], acc[a][c][1]), cvtpk(acc[a][c][2], acc[a][c][3])};
  WAVE_SYNC();
  u32x4 v[5];
#pragma unroll
  for (int e = 0; e < 5; ++e) v[e] = *(const __attribute__((address_space(3))) u32x4*)(uintptr_t)(sr + e * 1024);
#pragma unroll
  for (int e = 0; e < 5; ++e) __builtin_amdgcn_raw_buffer_store_b128(v[e], srdO, oo[e], 0, AUX3);
  STAMP_END();
}

extern "C" void kernel_launch(void* const* d_in, const int* in_sizes, int n_in, void* d_out, int out_size, void* d_ws, size_t ws_size, hipStream_t stream) {
  (void)in_sizes; (void)n_in; (void)out_size; (void)ws_size;
  (void)hipFuncSetAttribute((const void*)k1_qkv, hipFuncAttributeMaxDynamicSharedMemorySize, K1_LDS + STAMP_EXTRA);
  (void)hipFuncSetAttribute((const void*)k2_attn, hipFuncAttributeMaxDynamicSharedMemorySize, K2_LDS + STAMP_EXTRA);
  (void)hipFuncSetAttribute((const void*)k3_out, hipFuncAttributeMaxDynamicSharedMemorySize, K3_LDS + STAMP_EXTRA);
  char* ws = (char*)d_ws;
  for (int rep = 0; rep < REP1; ++rep)
  k1_qkv<<<256, 768, K1_LDS + STAMP_EXTRA, stream>>>((const char*)d_in[0], (const char*)d_in[1], (const char*)d_in[3], (const char*)d_in[4], ws, (const char*)d_in[2], (const char*)d_in[5], (const char*)d_out);
  for (int rep = 0; rep < REP2; ++rep)
  k2_attn<<<256, 576, K2_LDS + STAMP_EXTRA, stream>>>(ws, (const char*)d_in[2], (const char*)d_in[5]);
  for (int rep = 0; rep < REP3; ++rep)
  k3_out<<<256, 512, K3_LDS + STAMP_EXTRA, stream>>>((const char*)d_in[2], (const char*)d_in[5], ws, (char*)d_out);
}
```
